```python
import torch

def _softmax(x, axis=-1):
    return torch.softmax(x, dim=axis)
def _gelu(x):
    return torch.nn.functional.gelu(x, approximate="tanh")
def _silu(x):
    return torch.nn.functional.silu(x)
def _sigmoid(x):
    return torch.sigmoid(x)
def _layernorm(x, axis=-1, eps=1e-5):
    m = x.mean(dim=axis, keepdim=True)
    v = x.var(dim=axis, keepdim=True, unbiased=False)
    return (x - m) / torch.sqrt(v + eps)
def _scatter_add(tgt, idx, src, axis=0):
    return tgt.clone().index_add_(axis, idx, src)
def _segment_sum(src, seg, n):
    out = torch.zeros((n,) + src.shape[1:], dtype=src.dtype)
    return out.index_add_(0, seg, src)
def _pad(x, pw):
    flat = tuple(v for pair in reversed(pw) for v in pair)
    return torch.nn.functional.pad(x, flat)

kernel_name = 'sequential_preLN__spectral_gfnet__moe_arch_mod__prompt'

def setup_inputs():

    _w = (lambda a: (lambda t: t.to(torch.bfloat16) if t.is_floating_point() else t)(torch.as_tensor(a)))
    def _A_setup():
        B, S, D = 1, 2048, 576
        Df = D // 2 + 1
        return {
            "x": _w((lambda *s: torch.randn(*s, dtype=torch.bfloat16) * 0.05)(B, S, D)),
            "w_r": _w((lambda *s: torch.randn(*s, dtype=torch.bfloat16) * 0.8)(S, Df) * 0.02),
            "w_i": _w((lambda *s: torch.randn(*s, dtype=torch.bfloat16) * 0.8)(S, Df) * 0.02),
        }
    _A_in = _A_setup()

    def _B_setup():
        B, S, D, F, C = 1, 2048, 576, 4608, 8
        return {
            "x": _w((lambda *s: torch.randn(*s, dtype=torch.bfloat16) * 0.05)(B, S, D)),
            "w_r": _w((lambda *s: torch.randn(*s, dtype=torch.bfloat16) * 0.2)(D)),
            "w_gate": _w((lambda *s: torch.randn(*s, dtype=torch.bfloat16) * 0.2)(D, F)),
            "w_up": _w((lambda *s: torch.randn(*s, dtype=torch.bfloat16) * 0.2)(D, F)),
            "w_down": _w((lambda *s: torch.randn(*s, dtype=torch.bfloat16) * 0.2)(F, D)),
        }
    _B_in = _B_setup()

    return {
        "A_x": _A_in["x"],
        "A_w_r": _A_in["w_r"],
        "A_w_i": _A_in["w_i"],
        "B_x": _B_in["x"],
        "B_w_r": _B_in["w_r"],
        "B_w_gate": _B_in["w_gate"],
        "B_w_up": _B_in["w_up"],
        "B_w_down": _B_in["w_down"],
    }

def reference(A_x, A_w_r, A_w_i, B_x, B_w_r, B_w_gate, B_w_up, B_w_down):
    def _A_ref(x, w_r, w_i):
        B, S, D = x.shape
        X = (lambda x, axes=(-2,-1): torch.fft.rfft2(x.float(), dim=axes))(x, axes=(-2, -1))
        W = w_r + 1j * w_i
        X = X * W
        y = (lambda x, s, axes=(-2,-1): torch.fft.irfft2(x, s=s, dim=axes).to(torch.bfloat16))(X, (S, D), axes=(-2, -1))
        return y

    def _B_ref(x, w_r, w_gate, w_up, w_down):
        B, S, D = x.shape
        C = 8
        r = x @ w_r
        idx = torch.argsort(-r, axis=-1)[:, :C]
        idx_e = (lambda x, n, axis: torch.repeat_interleave(x, n, dim=axis))(idx.reshape(B, C, 1), D, axis=2)
        x_sel = (lambda x, i, axis: torch.take_along_dim(x, i, dim=axis))(x, idx_e, axis=1)
        g = _silu(x_sel @ w_gate)
        u = x_sel @ w_up
        y = (g * u) @ w_down
        r_sel = _sigmoid((lambda x, i, axis: torch.take_along_dim(x, i, dim=axis))(r, idx, axis=1))
        y = y * r_sel.reshape(B, C, 1)
        b_off = (torch.arange(B) * S).reshape(B, 1)
        flat_idx = (idx + b_off).reshape(B * C)
        zero = (x * 0.0).reshape(B * S, D)
        upd = _scatter_add(zero, flat_idx, y.reshape(B * C, D), axis=0)
        return x + upd.reshape(B, S, D)

    x = A_x
    h = x + _A_ref(_layernorm(x, axis=-1), A_w_r, A_w_i)
    h2 = h + _B_ref(_layernorm(h, axis=-1), B_w_r, B_w_gate, B_w_up, B_w_down)
    return h2
```

```cpp
#include <hip/hip_runtime.h>
#include <hip/hip_bf16.h>
#include <stdint.h>

typedef float f2 __attribute__((ext_vector_type(2)));
typedef float f4 __attribute__((ext_vector_type(4)));
typedef short s8 __attribute__((ext_vector_type(8)));
typedef unsigned int u32;
typedef unsigned short u16;
typedef u32 u32x2 __attribute__((ext_vector_type(2)));
typedef u32 u32x4 __attribute__((ext_vector_type(4)));

__device__ const __attribute__((aligned(16))) float g_tabR[1408] = {
1.f,0.f,1.f,0.f,1.f,0.f,1.f,0.f,1.f,0.f,1.f,0.f,
1.f,0.f,1.f,0.f,1.f,0.f,1.f,0.f,1.f,0.f,1.f,0.f,
1.f,0.f,1.f,0.f,1.f,0.f,1.f,0.f,1.f,0.f,1.f,0.f,
1.f,0.f,1.f,0.f,1.f,0.f,1.f,0.f,1.f,0.f,1.f,0.f,
1.f,0.f,1.f,0.f,1.f,0.f,1.f,0.f,1.f,0.f,1.f,0.f,
1.f,0.f,1.f,0.f,1.f,0.f,1.f,0.f,1.f,0.f,1.f,0.f,
1.f,0.f,0.999761999f,-0.0218148846f,0.999048233f,-0.0436193869f,0.997858942f,-0.0654031262f,0.99619472f,-0.0871557444f,0.994056344f,-0.108866878f,
0.991444886f,-0.130526185f,0.988361537f,-0.152123392f,0.98480773f,-0.173648179f,0.980785251f,-0.195090324f,0.976296008f,-0.21643962f,0.971342087f,-0.237685889f,
0.965925813f,-0.258819044f,0.960049868f,-0.279829025f,0.953716934f,-0.300705791f,0.94693011f,-0.321439475f,0.939692616f,-0.342020154f,0.932007849f,-0.362438053f,
0.923879504f,-0.382683426f,0.915311456f,-0.402746677f,0.906307817f,-0.42261827f,0.896872759f,-0.442288697f,0.887010813f,-0.4617486f,0.876726747f,-0.480988771f,
0.866025388f,-0.5f,0.854911864f,-0.518773258f,0.843391418f,-0.537299633f,0.831469595f,-0.555570245f,0.819152057f,-0.57357645f,0.806444585f,-0.591309667f,
0.793353319f,-0.60876143f,0.779884458f,-0.625923455f,0.766044438f,-0.642787635f,0.751839817f,-0.659345806f,0.737277329f,-0.675590217f,0.722363949f,-0.691513062f,
1.f,0.f,0.999048233f,-0.0436193869f,0.99619472f,-0.0871557444f,0.991444886f,-0.130526185f,0.98480773f,-0.173648179f,0.976296008f,-0.21643962f,
0.965925813f,-0.258819044f,0.953716934f,-0.300705791f,0.939692616f,-0.342020154f,0.923879504f,-0.382683426f,0.906307817f,-0.42261827f,0.887010813f,-0.4617486f,
0.866025388f,-0.5f,0.843391418f,-0.537299633f,0.819152057f,-0.57357645f,0.793353319f,-0.60876143f,0.766044438f,-0.642787635f,0.737277329f,-0.675590217f,
0.707106769f,-0.707106769f,0.675590217f,-0.737277329f,0.642787635f,-0.766044438f,0.60876143f,-0.793353319f,0.57357645f,-0.819152057f,0.537299633f,-0.843391418f,
0.5f,-0.866025388f,0.4617486f,-0.887010813f,0.42261827f,-0.906307817f,0.382683426f,-0.923879504f,0.342020154f,-0.939692616f,0.300705791f,-0.953716934f,
0.258819044f,-0.965925813f,0.21643962f,-0.976296008f,0.173648179f,-0.98480773f,0.130526185f,-0.991444886f,0.0871557444f,-0.99619472f,0.0436193869f,-0.999048233f,
1.f,0.f,0.997858942f,-0.0654031262f,0.991444886f,-0.130526185f,0.980785251f,-0.195090324f,0.965925813f,-0.258819044f,0.94693011f,-0.321439475f,
0.923879504f,-0.382683426f,0.896872759f,-0.442288697f,0.866025388f,-0.5f,0.831469595f,-0.555570245f,0.793353319f,-0.60876143f,0.751839817f,-0.659345806f,
0.707106769f,-0.707106769f,0.659345806f,-0.751839817f,0.60876143f,-0.793353319f,0.555570245f,-0.831469595f,0.5f,-0.866025388f,0.442288697f,-0.896872759f,
0.382683426f,-0.923879504f,0.321439475f,-0.94693011f,0.258819044f,-0.965925813f,0.195090324f,-0.980785251f,0.130526185f,-0.991444886f,0.0654031262f,-0.997858942f,
0.f,-1.f,-0.0654031262f,-0.997858942f,-0.130526185f,-0.991444886f,-0.195090324f,-0.980785251f,-0.258819044f,-0.965925813f,-0.321439475f,-0.94693011f,
-0.382683426f,-0.923879504f,-0.442288697f,-0.896872759f,-0.5f,-0.866025388f,-0.555570245f,-0.831469595f,-0.60876143f,-0.793353319f,-0.659345806f,-0.751839817f,
1.f,0.f,0.99619472f,-0.0871557444f,0.98480773f,-0.173648179f,0.965925813f,-0.258819044f,0.939692616f,-0.342020154f,0.906307817f,-0.42261827f,
0.866025388f,-0.5f,0.819152057f,-0.57357645f,0.766044438f,-0.642787635f,0.707106769f,-0.707106769f,0.642787635f,-0.766044438f,0.57357645f,-0.819152057f,
0.5f,-0.866025388f,0.42261827f,-0.906307817f,0.342020154f,-0.939692616f,0.258819044f,-0.965925813f,0.173648179f,-0.98480773f,0.0871557444f,-0.99619472f,
0.f,-1.f,-0.0871557444f,-0.99619472f,-0.173648179f,-0.98480773f,-0.258819044f,-0.965925813f,-0.342020154f,-0.939692616f,-0.42261827f,-0.906307817f,
-0.5f,-0.866025388f,-0.57357645f,-0.819152057f,-0.642787635f,-0.766044438f,-0.707106769f,-0.707106769f,-0.766044438f,-0.642787635f,-0.819152057f,-0.57357645f,
-0.866025388f,-0.5f,-0.906307817f,-0.42261827f,-0.939692616f,-0.342020154f,-0.965925813f,-0.258819044f,-0.98480773f,-0.173648179f,-0.99619472f,-0.0871557444f,
1.f,0.f,0.994056344f,-0.108866878f,0.976296008f,-0.21643962f,0.94693011f,-0.321439475f,0.906307817f,-0.42261827f,0.854911864f,-0.518773258f,
0.793353319f,-0.60876143f,0.722363949f,-0.691513062f,0.642787635f,-0.766044438f,0.555570245f,-0.831469595f,0.4617486f,-0.887010813f,0.362438053f,-0.932007849f,
0.258819044f,-0.965925813f,0.152123392f,-0.988361537f,0.0436193869f,-0.999048233f,-0.0654031262f,-0.997858942f,-0.173648179f,-0.98480773f,-0.279829025f,-0.960049868f,
-0.382683426f,-0.923879504f,-0.480988771f,-0.876726747f,-0.57357645f,-0.819152057f,-0.659345806f,-0.751839817f,-0.737277329f,-0.675590217f,-0.806444585f,-0.591309667f,
-0.866025388f,-0.5f,-0.915311456f,-0.402746677f,-0.953716934f,-0.300705791f,-0.980785251f,-0.195090324f,-0.99619472f,-0.0871557444f,-0.999761999f,0.0218148846f,
-0.991444886f,0.130526185f,-0.971342087f,0.237685889f,-0.939692616f,0.342020154f,-0.896872759f,0.442288697f,-0.843391418f,0.537299633f,-0.779884458f,0.625923455f,
1.f,0.f,0.991444886f,-0.130526185f,0.965925813f,-0.258819044f,0.923879504f,-0.382683426f,0.866025388f,-0.5f,0.793353319f,-0.60876143f,
0.707106769f,-0.707106769f,0.60876143f,-0.793353319f,0.5f,-0.866025388f,0.382683426f,-0.923879504f,0.258819044f,-0.965925813f,0.130526185f,-0.991444886f,
0.f,-1.f,-0.130526185f,-0.991444886f,-0.258819044f,-0.965925813f,-0.382683426f,-0.923879504f,-0.5f,-0.866025388f,-0.60876143f,-0.793353319f,
-0.707106769f,-0.707106769f,-0.793353319f,-0.60876143f,-0.866025388f,-0.5f,-0.923879504f,-0.382683426f,-0.965925813f,-0.258819044f,-0.991444886f,-0.130526185f,
-1.f,0.f,-0.991444886f,0.130526185f,-0.965925813f,0.258819044f,-0.923879504f,0.382683426f,-0.866025388f,0.5f,-0.793353319f,0.60876143f,
-0.707106769f,0.707106769f,-0.60876143f,0.793353319f,-0.5f,0.866025388f,-0.382683426f,0.923879504f,-0.258819044f,0.965925813f,-0.130526185f,0.991444886f,
1.f,0.f,0.988361537f,-0.152123392f,0.953716934f,-0.300705791f,0.896872759f,-0.442288697f,0.819152057f,-0.57357645f,0.722363949f,-0.691513062f,
0.60876143f,-0.793353319f,0.480988771f,-0.876726747f,0.342020154f,-0.939692616f,0.195090324f,-0.980785251f,0.0436193869f,-0.999048233f,-0.108866878f,-0.994056344f,
-0.258819044f,-0.965925813f,-0.402746677f,-0.915311456f,-0.537299633f,-0.843391418f,-0.659345806f,-0.751839817f,-0.766044438f,-0.642787635f,-0.854911864f,-0.518773258f,
-0.923879504f,-0.382683426f,-0.971342087f,-0.237685889f,-0.99619472f,-0.0871557444f,-0.997858942f,0.0654031262f,-0.976296008f,0.21643962f,-0.932007849f,0.362438053f,
-0.866025388f,0.5f,-0.779884458f,0.625923455f,-0.675590217f,0.737277329f,-0.555570245f,0.831469595f,-0.42261827f,0.906307817f,-0.279829025f,0.960049868f,
-0.130526185f,0.991444886f,0.0218148846f,0.999761999f,0.173648179f,0.98480773f,0.321439475f,0.94693011f,0.4617486f,0.887010813f,0.591309667f,0.806444585f,
1.f,0.f,0.98480773f,-0.173648179f,0.939692616f,-0.342020154f,0.866025388f,-0.5f,0.766044438f,-0.642787635f,0.642787635f,-0.766044438f,
0.5f,-0.866025388f,0.342020154f,-0.939692616f,0.173648179f,-0.98480773f,0.f,-1.f,-0.173648179f,-0.98480773f,-0.342020154f,-0.939692616f,
-0.5f,-0.866025388f,-0.642787635f,-0.766044438f,-0.766044438f,-0.642787635f,-0.866025388f,-0.5f,-0.939692616f,-0.342020154f,-0.98480773f,-0.173648179f,
-1.f,0.f,-0.98480773f,0.173648179f,-0.939692616f,0.342020154f,-0.866025388f,0.5f,-0.766044438f,0.642787635f,-0.642787635f,0.766044438f,
-0.5f,0.866025388f,-0.342020154f,0.939692616f,-0.173648179f,0.98480773f,0.f,1.f,0.173648179f,0.98480773f,0.342020154f,0.939692616f,
0.5f,0.866025388f,0.642787635f,0.766044438f,0.766044438f,0.642787635f,0.866025388f,0.5f,0.939692616f,0.342020154f,0.98480773f,0.173648179f,
1.f,0.f,1.f,0.f,1.f,0.f,1.f,0.f,1.f,0.f,0.980785251f,-0.195090324f,
0.923879504f,-0.382683426f,0.831469595f,-0.555570245f,1.f,0.f,0.923879504f,-0.382683426f,0.707106769f,-0.707106769f,0.382683426f,-0.923879504f,
1.f,0.f,0.831469595f,-0.555570245f,0.382683426f,-0.923879504f,-0.195090324f,-0.980785251f,1.f,0.f,0.707106769f,-0.707106769f,
0.f,-1.f,-0.707106769f,-0.707106769f,1.f,0.f,0.555570245f,-0.831469595f,-0.382683426f,-0.923879504f,-0.980785251f,-0.195090324f,
1.f,0.f,0.382683426f,-0.923879504f,-0.707106769f,-0.707106769f,-0.923879504f,0.382683426f,1.f,0.f,0.195090324f,-0.980785251f,
-0.923879504f,-0.382683426f,-0.555570245f,0.831469595f,1.f,0.f,0.999940515f,-0.0109080914f,0.999761999f,-0.0218148846f,0.999464571f,-0.0327190831f,
0.999048233f,-0.0436193869f,0.998512983f,-0.0545145012f,0.997858942f,-0.0654031262f,0.997086108f,-0.0762839764f,0.99619472f,-0.0871557444f,0.99518472f,-0.0980171412f,
0.994056344f,-0.108866878f,0.992809653f,-0.119703658f,0.991444886f,-0.130526185f,0.989962101f,-0.141333193f,0.988361537f,-0.152123392f,0.986643314f,-0.162895471f,
0.98480773f,-0.173648179f,0.982854962f,-0.184380218f,0.980785251f,-0.195090324f,0.978598893f,-0.205777213f,0.976296008f,-0.21643962f,0.973876953f,-0.227076262f,
0.971342087f,-0.237685889f,0.968691587f,-0.248267233f,0.965925813f,-0.258819044f,0.96304512f,-0.269340068f,0.960049868f,-0.279829025f,0.956940353f,-0.290284663f,
0.953716934f,-0.300705791f,0.950380087f,-0.311091155f,0.94693011f,-0.321439475f,0.943367481f,-0.331749529f,0.939692616f,-0.342020154f,0.935905933f,-0.35225004f,
0.932007849f,-0.362438053f,0.9279989f,-0.372582912f,0.923879504f,-0.382683426f,0.919650197f,-0.392738432f,0.915311456f,-0.402746677f,0.910863817f,-0.412707031f,
0.906307817f,-0.42261827f,0.901643932f,-0.432479203f,0.896872759f,-0.442288697f,0.891994834f,-0.45204556f,0.887010813f,-0.4617486f,0.881921291f,-0.471396744f,
0.876726747f,-0.480988771f,0.871427953f,-0.490523577f,0.866025388f,-0.5f,0.860519826f,-0.509416938f,0.854911864f,-0.518773258f,0.849202156f,-0.528067827f,
0.843391418f,-0.537299633f,0.837480366f,-0.546467423f,0.831469595f,-0.555570245f,0.825359941f,-0.564606905f,0.819152057f,-0.57357645f,0.812846661f,-0.582477689f,
0.806444585f,-0.591309667f,0.799946547f,-0.600071251f,0.793353319f,-0.60876143f,0.786665738f,-0.617379189f,0.779884458f,-0.625923455f,0.773010433f,-0.634393275f,
0.766044438f,-0.642787635f,0.758987308f,-0.651105464f,0.751839817f,-0.659345806f,0.744602859f,-0.667507708f,0.737277329f,-0.675590217f,0.729864061f,-0.683592319f,
0.722363949f,-0.691513062f,0.714777887f,-0.699351549f,0.707106769f,-0.707106769f,0.699351549f,-0.714777887f,0.691513062f,-0.722363949f,0.683592319f,-0.729864061f,
0.675590217f,-0.737277329f,0.667507708f,-0.744602859f,0.659345806f,-0.751839817f,0.651105464f,-0.758987308f,0.642787635f,-0.766044438f,0.634393275f,-0.773010433f,
0.625923455f,-0.779884458f,0.617379189f,-0.786665738f,0.60876143f,-0.793353319f,0.600071251f,-0.799946547f,0.591309667f,-0.806444585f,0.582477689f,-0.812846661f,
0.57357645f,-0.819152057f,0.564606905f,-0.825359941f,0.555570245f,-0.831469595f,0.546467423f,-0.837480366f,0.537299633f,-0.843391418f,0.528067827f,-0.849202156f,
0.518773258f,-0.854911864f,0.509416938f,-0.860519826f,0.5f,-0.866025388f,0.490523577f,-0.871427953f,0.480988771f,-0.876726747f,0.471396744f,-0.881921291f,
0.4617486f,-0.887010813f,0.45204556f,-0.891994834f,0.442288697f,-0.896872759f,0.432479203f,-0.901643932f,0.42261827f,-0.906307817f,0.412707031f,-0.910863817f,
0.402746677f,-0.915311456f,0.392738432f,-0.919650197f,0.382683426f,-0.923879504f,0.372582912f,-0.9279989f,0.362438053f,-0.932007849f,0.35225004f,-0.935905933f,
0.342020154f,-0.939692616f,0.331749529f,-0.943367481f,0.321439475f,-0.94693011f,0.311091155f,-0.950380087f,0.300705791f,-0.953716934f,0.290284663f,-0.956940353f,
0.279829025f,-0.960049868f,0.269340068f,-0.96304512f,0.258819044f,-0.965925813f,0.248267233f,-0.968691587f,0.237685889f,-0.971342087f,0.227076262f,-0.973876953f,
0.21643962f,-0.976296008f,0.205777213f,-0.978598893f,0.195090324f,-0.980785251f,0.184380218f,-0.982854962f,0.173648179f,-0.98480773f,0.162895471f,-0.986643314f,
0.152123392f,-0.988361537f,0.141333193f,-0.989962101f,0.130526185f,-0.991444886f,0.119703658f,-0.992809653f,0.108866878f,-0.994056344f,0.0980171412f,-0.99518472f,
0.0871557444f,-0.99619472f,0.0762839764f,-0.997086108f,0.0654031262f,-0.997858942f,0.0545145012f,-0.998512983f,0.0436193869f,-0.999048233f,0.0327190831f,-0.999464571f,
0.0218148846f,-0.999761999f,0.0109080914f,-0.999940515f,0.f,-1.f,-0.0109080914f,-0.999940515f,-0.0218148846f,-0.999761999f,-0.0327190831f,-0.999464571f,
-0.0436193869f,-0.999048233f,-0.0545145012f,-0.998512983f,-0.0654031262f,-0.997858942f,-0.0762839764f,-0.997086108f,-0.0871557444f,-0.99619472f,-0.0980171412f,-0.99518472f,
-0.108866878f,-0.994056344f,-0.119703658f,-0.992809653f,-0.130526185f,-0.991444886f,-0.141333193f,-0.989962101f,-0.152123392f,-0.988361537f,-0.162895471f,-0.986643314f,
-0.173648179f,-0.98480773f,-0.184380218f,-0.982854962f,-0.195090324f,-0.980785251f,-0.205777213f,-0.978598893f,-0.21643962f,-0.976296008f,-0.227076262f,-0.973876953f,
-0.237685889f,-0.971342087f,-0.248267233f,-0.968691587f,-0.258819044f,-0.965925813f,-0.269340068f,-0.96304512f,-0.279829025f,-0.960049868f,-0.290284663f,-0.956940353f,
-0.300705791f,-0.953716934f,-0.311091155f,-0.950380087f,-0.321439475f,-0.94693011f,-0.331749529f,-0.943367481f,-0.342020154f,-0.939692616f,-0.35225004f,-0.935905933f,
-0.362438053f,-0.932007849f,-0.372582912f,-0.9279989f,-0.382683426f,-0.923879504f,-0.392738432f,-0.919650197f,-0.402746677f,-0.915311456f,-0.412707031f,-0.910863817f,
-0.42261827f,-0.906307817f,-0.432479203f,-0.901643932f,-0.442288697f,-0.896872759f,-0.45204556f,-0.891994834f,-0.4617486f,-0.887010813f,-0.471396744f,-0.881921291f,
-0.480988771f,-0.876726747f,-0.490523577f,-0.871427953f,-0.5f,-0.866025388f,-0.509416938f,-0.860519826f,-0.518773258f,-0.854911864f,-0.528067827f,-0.849202156f,
-0.537299633f,-0.843391418f,-0.546467423f,-0.837480366f,-0.555570245f,-0.831469595f,-0.564606905f,-0.825359941f,-0.57357645f,-0.819152057f,-0.582477689f,-0.812846661f,
-0.591309667f,-0.806444585f,-0.600071251f,-0.799946547f,-0.60876143f,-0.793353319f,-0.617379189f,-0.786665738f,-0.625923455f,-0.779884458f,-0.634393275f,-0.773010433f,
-0.642787635f,-0.766044438f,-0.651105464f,-0.758987308f,-0.659345806f,-0.751839817f,-0.667507708f,-0.744602859f,-0.675590217f,-0.737277329f,-0.683592319f,-0.729864061f,
-0.691513062f,-0.722363949f,-0.699351549f,-0.714777887f,-0.707106769f,-0.707106769f,-0.714777887f,-0.699351549f,-0.722363949f,-0.691513062f,-0.729864061f,-0.683592319f,
-0.737277329f,-0.675590217f,-0.744602859f,-0.667507708f,-0.751839817f,-0.659345806f,-0.758987308f,-0.651105464f,-0.766044438f,-0.642787635f,-0.773010433f,-0.634393275f,
-0.779884458f,-0.625923455f,-0.786665738f,-0.617379189f,-0.793353319f,-0.60876143f,-0.799946547f,-0.600071251f,-0.806444585f,-0.591309667f,-0.812846661f,-0.582477689f,
-0.819152057f,-0.57357645f,-0.825359941f,-0.564606905f,-0.831469595f,-0.555570245f,-0.837480366f,-0.546467423f,-0.843391418f,-0.537299633f,-0.849202156f,-0.528067827f,
-0.854911864f,-0.518773258f,-0.860519826f,-0.509416938f,-0.866025388f,-0.5f,-0.871427953f,-0.490523577f,-0.876726747f,-0.480988771f,-0.881921291f,-0.471396744f,
-0.887010813f,-0.4617486f,-0.891994834f,-0.45204556f,-0.896872759f,-0.442288697f,-0.901643932f,-0.432479203f,-0.906307817f,-0.42261827f,-0.910863817f,-0.412707031f,
-0.915311456f,-0.402746677f,-0.919650197f,-0.392738432f,-0.923879504f,-0.382683426f,-0.9279989f,-0.372582912f,-0.932007849f,-0.362438053f,-0.935905933f,-0.35225004f,
-0.939692616f,-0.342020154f,-0.943367481f,-0.331749529f,-0.94693011f,-0.321439475f,-0.950380087f,-0.311091155f,-0.953716934f,-0.300705791f,-0.956940353f,-0.290284663f,
-0.960049868f,-0.279829025f,-0.96304512f,-0.269340068f,-0.965925813f,-0.258819044f,-0.968691587f,-0.248267233f,-0.971342087f,-0.237685889f,-0.973876953f,-0.227076262f,
-0.976296008f,-0.21643962f,-0.978598893f,-0.205777213f,-0.980785251f,-0.195090324f,-0.982854962f,-0.184380218f,-0.98480773f,-0.173648179f,-0.986643314f,-0.162895471f,
-0.988361537f,-0.152123392f,-0.989962101f,-0.141333193f,-0.991444886f,-0.130526185f,-0.992809653f,-0.119703658f,-0.994056344f,-0.108866878f,-0.99518472f,-0.0980171412f,
-0.99619472f,-0.0871557444f,-0.997086108f,-0.0762839764f,-0.997858942f,-0.0654031262f,-0.998512983f,-0.0545145012f,-0.999048233f,-0.0436193869f,-0.999464571f,-0.0327190831f,
-0.999761999f,-0.0218148846f,-0.999940515f,-0.0109080914f,-1.f,0.f,-0.999940515f,0.0109080914f,-0.999761999f,0.0218148846f,-0.999464571f,0.0327190831f,
-0.999048233f,0.0436193869f,-0.998512983f,0.0545145012f,-0.997858942f,0.0654031262f,-0.997086108f,0.0762839764f,-0.99619472f,0.0871557444f,-0.99518472f,0.0980171412f,
-0.994056344f,0.108866878f,-0.992809653f,0.119703658f,-0.991444886f,0.130526185f,-0.989962101f,0.141333193f,-0.988361537f,0.152123392f,-0.986643314f,0.162895471f,
-0.98480773f,0.173648179f,-0.982854962f,0.184380218f,-0.980785251f,0.195090324f,-0.978598893f,0.205777213f,-0.976296008f,0.21643962f,-0.973876953f,0.227076262f,
-0.971342087f,0.237685889f,-0.968691587f,0.248267233f,-0.965925813f,0.258819044f,-0.96304512f,0.269340068f,-0.960049868f,0.279829025f,-0.956940353f,0.290284663f,
-0.953716934f,0.300705791f,-0.950380087f,0.311091155f,-0.94693011f,0.321439475f,-0.943367481f,0.331749529f,0.f,0.f,0.f,0.f,
0.f,0.f,0.f,0.f,0.f,0.f,0.f,0.f,0.f,0.f,0.f,0.f,
0.f,0.f,0.f,0.f,0.f,0.f,0.f,0.f,0.f,0.f,0.f,0.f,
0.f,0.f,0.f,0.f,0.f,0.f,0.f,0.f,0.f,0.f,0.f,0.f,
0.f,0.f,0.f,0.f,0.f,0.f,0.f,0.f,0.f,0.f,0.f,0.f,
0.f,0.f,0.f,0.f
};
__device__ const __attribute__((aligned(16))) float g_tabC[640] = {
1.f,0.f,1.f,0.f,1.f,0.f,1.f,0.f,1.f,0.f,1.f,0.f,
1.f,0.f,1.f,0.f,1.f,0.f,1.f,0.f,1.f,0.f,1.f,0.f,
1.f,0.f,1.f,0.f,1.f,0.f,1.f,0.f,1.f,0.f,1.f,0.f,
1.f,0.f,1.f,0.f,1.f,0.f,1.f,0.f,1.f,0.f,1.f,0.f,
1.f,0.f,1.f,0.f,1.f,0.f,1.f,0.f,1.f,0.f,1.f,0.f,
1.f,0.f,1.f,0.f,1.f,0.f,1.f,0.f,1.f,0.f,1.f,0.f,
1.f,0.f,0.999698818f,-0.024541229f,0.99879545f,-0.0490676761f,0.997290432f,-0.0735645667f,0.99518472f,-0.0980171412f,0.992479563f,-0.122410677f,
0.989176512f,-0.146730468f,0.985277653f,-0.170961887f,0.980785251f,-0.195090324f,0.975702107f,-0.219101235f,0.970031261f,-0.242980182f,0.963776052f,-0.266712755f,
0.956940353f,-0.290284663f,0.949528158f,-0.313681751f,0.941544056f,-0.336889863f,0.932992816f,-0.359895051f,0.923879504f,-0.382683426f,0.914209783f,-0.405241311f,
0.903989315f,-0.427555084f,0.893224299f,-0.449611336f,0.881921291f,-0.471396744f,0.870086968f,-0.492898196f,0.857728601f,-0.514102757f,0.84485358f,-0.534997642f,
0.831469595f,-0.555570245f,0.817584813f,-0.575808167f,0.803207517f,-0.59569931f,0.78834641f,-0.615231574f,0.773010433f,-0.634393275f,0.757208824f,-0.653172851f,
0.740951121f,-0.671558976f,0.724247098f,-0.689540565f,0.707106769f,-0.707106769f,0.689540565f,-0.724247098f,0.671558976f,-0.740951121f,0.653172851f,-0.757208824f,
1.f,0.f,0.99879545f,-0.0490676761f,0.99518472f,-0.0980171412f,0.989176512f,-0.146730468f,0.980785251f,-0.195090324f,0.970031261f,-0.242980182f,
0.956940353f,-0.290284663f,0.941544056f,-0.336889863f,0.923879504f,-0.382683426f,0.903989315f,-0.427555084f,0.881921291f,-0.471396744f,0.857728601f,-0.514102757f,
0.831469595f,-0.555570245f,0.803207517f,-0.59569931f,0.773010433f,-0.634393275f,0.740951121f,-0.671558976f,0.707106769f,-0.707106769f,0.671558976f,-0.740951121f,
0.634393275f,-0.773010433f,0.59569931f,-0.803207517f,0.555570245f,-0.831469595f,0.514102757f,-0.857728601f,0.471396744f,-0.881921291f,0.427555084f,-0.903989315f,
0.382683426f,-0.923879504f,0.336889863f,-0.941544056f,0.290284663f,-0.956940353f,0.242980182f,-0.970031261f,0.195090324f,-0.980785251f,0.146730468f,-0.989176512f,
0.0980171412f,-0.99518472f,0.0490676761f,-0.99879545f,0.f,-1.f,-0.0490676761f,-0.99879545f,-0.0980171412f,-0.99518472f,-0.146730468f,-0.989176512f,
1.f,0.f,0.997290432f,-0.0735645667f,0.989176512f,-0.146730468f,0.975702107f,-0.219101235f,0.956940353f,-0.290284663f,0.932992816f,-0.359895051f,
0.903989315f,-0.427555084f,0.870086968f,-0.492898196f,0.831469595f,-0.555570245f,0.78834641f,-0.615231574f,0.740951121f,-0.671558976f,0.689540565f,-0.724247098f,
0.634393275f,-0.773010433f,0.575808167f,-0.817584813f,0.514102757f,-0.857728601f,0.449611336f,-0.893224299f,0.382683426f,-0.923879504f,0.313681751f,-0.949528158f,
0.242980182f,-0.970031261f,0.170961887f,-0.985277653f,0.0980171412f,-0.99518472f,0.024541229f,-0.999698818f,-0.0490676761f,-0.99879545f,-0.122410677f,-0.992479563f,
-0.195090324f,-0.980785251f,-0.266712755f,-0.963776052f,-0.336889863f,-0.941544056f,-0.405241311f,-0.914209783f,-0.471396744f,-0.881921291f,-0.534997642f,-0.84485358f,
-0.59569931f,-0.803207517f,-0.653172851f,-0.757208824f,-0.707106769f,-0.707106769f,-0.757208824f,-0.653172851f,-0.803207517f,-0.59569931f,-0.84485358f,-0.534997642f,
1.f,0.f,0.99518472f,-0.0980171412f,0.980785251f,-0.195090324f,0.956940353f,-0.290284663f,0.923879504f,-0.382683426f,0.881921291f,-0.471396744f,
0.831469595f,-0.555570245f,0.773010433f,-0.634393275f,0.707106769f,-0.707106769f,0.634393275f,-0.773010433f,0.555570245f,-0.831469595f,0.471396744f,-0.881921291f,
0.382683426f,-0.923879504f,0.290284663f,-0.956940353f,0.195090324f,-0.980785251f,0.0980171412f,-0.99518472f,0.f,-1.f,-0.0980171412f,-0.99518472f,
-0.195090324f,-0.980785251f,-0.290284663f,-0.956940353f,-0.382683426f,-0.923879504f,-0.471396744f,-0.881921291f,-0.555570245f,-0.831469595f,-0.634393275f,-0.773010433f,
-0.707106769f,-0.707106769f,-0.773010433f,-0.634393275f,-0.831469595f,-0.555570245f,-0.881921291f,-0.471396744f,-0.923879504f,-0.382683426f,-0.956940353f,-0.290284663f,
-0.980785251f,-0.195090324f,-0.99518472f,-0.0980171412f,-1.f,0.f,-0.99518472f,0.0980171412f,-0.980785251f,0.195090324f,-0.956940353f,0.290284663f,
1.f,0.f,0.992479563f,-0.122410677f,0.970031261f,-0.242980182f,0.932992816f,-0.359895051f,0.881921291f,-0.471396744f,0.817584813f,-0.575808167f,
0.740951121f,-0.671558976f,0.653172851f,-0.757208824f,0.555570245f,-0.831469595f,0.449611336f,-0.893224299f,0.336889863f,-0.941544056f,0.219101235f,-0.975702107f,
0.0980171412f,-0.99518472f,-0.024541229f,-0.999698818f,-0.146730468f,-0.989176512f,-0.266712755f,-0.963776052f,-0.382683426f,-0.923879504f,-0.492898196f,-0.870086968f,
-0.59569931f,-0.803207517f,-0.689540565f,-0.724247098f,-0.773010433f,-0.634393275f,-0.84485358f,-0.534997642f,-0.903989315f,-0.427555084f,-0.949528158f,-0.313681751f,
-0.980785251f,-0.195090324f,-0.997290432f,-0.0735645667f,-0.99879545f,0.0490676761f,-0.985277653f,0.170961887f,-0.956940353f,0.290284663f,-0.914209783f,0.405241311f,
-0.857728601f,0.514102757f,-0.78834641f,0.615231574f,-0.707106769f,0.707106769f,-0.615231574f,0.78834641f,-0.514102757f,0.857728601f,-0.405241311f,0.914209783f,
1.f,0.f,0.989176512f,-0.146730468f,0.956940353f,-0.290284663f,0.903989315f,-0.427555084f,0.831469595f,-0.555570245f,0.740951121f,-0.671558976f,
0.634393275f,-0.773010433f,0.514102757f,-0.857728601f,0.382683426f,-0.923879504f,0.242980182f,-0.970031261f,0.0980171412f,-0.99518472f,-0.0490676761f,-0.99879545f,
-0.195090324f,-0.980785251f,-0.336889863f,-0.941544056f,-0.471396744f,-0.881921291f,-0.59569931f,-0.803207517f,-0.707106769f,-0.707106769f,-0.803207517f,-0.59569931f,
-0.881921291f,-0.471396744f,-0.941544056f,-0.336889863f,-0.980785251f,-0.195090324f,-0.99879545f,-0.0490676761f,-0.99518472f,0.0980171412f,-0.970031261f,0.242980182f,
-0.923879504f,0.382683426f,-0.857728601f,0.514102757f,-0.773010433f,0.634393275f,-0.671558976f,0.740951121f,-0.555570245f,0.831469595f,-0.427555084f,0.903989315f,
-0.290284663f,0.956940353f,-0.146730468f,0.989176512f,0.f,1.f,0.146730468f,0.989176512f,0.290284663f,0.956940353f,0.427555084f,0.903989315f,
1.f,0.f,0.985277653f,-0.170961887f,0.941544056f,-0.336889863f,0.870086968f,-0.492898196f,0.773010433f,-0.634393275f,0.653172851f,-0.757208824f,
0.514102757f,-0.857728601f,0.359895051f,-0.932992816f,0.195090324f,-0.980785251f,0.024541229f,-0.999698818f,-0.146730468f,-0.989176512f,-0.313681751f,-0.949528158f,
-0.471396744f,-0.881921291f,-0.615231574f,-0.78834641f,-0.740951121f,-0.671558976f,-0.84485358f,-0.534997642f,-0.923879504f,-0.382683426f,-0.975702107f,-0.219101235f,
-0.99879545f,-0.0490676761f,-0.992479563f,0.122410677f,-0.956940353f,0.290284663f,-0.893224299f,0.449611336f,-0.803207517f,0.59569931f,-0.689540565f,0.724247098f,
-0.555570245f,0.831469595f,-0.405241311f,0.914209783f,-0.242980182f,0.970031261f,-0.0735645667f,0.997290432f,0.0980171412f,0.99518472f,0.266712755f,0.963776052f,
0.427555084f,0.903989315f,0.575808167f,0.817584813f,0.707106769f,0.707106769f,0.817584813f,0.575808167f,0.903989315f,0.427555084f,0.963776052f,0.266712755f,
1.f,0.f,1.f,0.f,1.f,0.f,1.f,0.f,1.f,0.f,0.980785251f,-0.195090324f,
0.923879504f,-0.382683426f,0.831469595f,-0.555570245f,1.f,0.f,0.923879504f,-0.382683426f,0.707106769f,-0.707106769f,0.382683426f,-0.923879504f,
1.f,0.f,0.831469595f,-0.555570245f,0.382683426f,-0.923879504f,-0.195090324f,-0.980785251f,1.f,0.f,0.707106769f,-0.707106769f,
0.f,-1.f,-0.707106769f,-0.707106769f,1.f,0.f,0.555570245f,-0.831469595f,-0.382683426f,-0.923879504f,-0.980785251f,-0.195090324f,
1.f,0.f,0.382683426f,-0.923879504f,-0.707106769f,-0.707106769f,-0.923879504f,0.382683426f,1.f,0.f,0.195090324f,-0.980785251f,
-0.923879504f,-0.382683426f,-0.555570245f,0.831469595f
};
__device__ const __attribute__((aligned(16))) float g_twF[4096] = {
1.f,0.f,1.f,0.f,1.f,0.f,1.f,0.f,1.f,0.f,1.f,0.f,
1.f,0.f,1.f,0.f,1.f,0.f,0.999995291f,-0.00306795677f,0.999981165f,-0.00613588467f,0.999957621f,-0.00920375437f,
0.999924719f,-0.0122715384f,0.99988234f,-0.015339206f,0.999830604f,-0.0184067301f,0.99976939f,-0.0214740802f,1.f,0.f,0.999981165f,-0.00613588467f,
0.999924719f,-0.0122715384f,0.999830604f,-0.0184067301f,0.999698818f,-0.024541229f,0.999529421f,-0.030674804f,0.999322355f,-0.0368072242f,0.999077737f,-0.0429382585f,
1.f,0.f,0.999957621f,-0.00920375437f,0.999830604f,-0.0184067301f,0.999618828f,-0.027608145f,0.999322355f,-0.0368072242f,0.998941302f,-0.0460031815f,
0.998475552f,-0.0551952459f,0.997925282f,-0.0643826276f,1.f,0.f,0.999924719f,-0.0122715384f,0.999698818f,-0.024541229f,0.999322355f,-0.0368072242f,
0.99879545f,-0.0490676761f,0.998118103f,-0.061320737f,0.997290432f,-0.0735645667f,0.996312618f,-0.0857973099f,1.f,0.f,0.99988234f,-0.015339206f,
0.999529421f,-0.030674804f,0.998941302f,-0.0460031815f,0.998118103f,-0.061320737f,0.997060061f,-0.0766238645f,0.995767415f,-0.0919089541f,0.994240463f,-0.107172422f,
1.f,0.f,0.999830604f,-0.0184067301f,0.999322355f,-0.0368072242f,0.998475552f,-0.0551952459f,0.997290432f,-0.0735645667f,0.995767415f,-0.0919089541f,
0.993906975f,-0.110222206f,0.991709769f,-0.128498107f,1.f,0.f,0.99976939f,-0.0214740802f,0.999077737f,-0.0429382585f,0.997925282f,-0.0643826276f,
0.996312618f,-0.0857973099f,0.994240463f,-0.107172422f,0.991709769f,-0.128498107f,0.988721669f,-0.149764538f,1.f,0.f,0.999698818f,-0.024541229f,
0.99879545f,-0.0490676761f,0.997290432f,-0.0735645667f,0.99518472f,-0.0980171412f,0.992479563f,-0.122410677f,0.989176512f,-0.146730468f,0.985277653f,-0.170961887f,
1.f,0.f,0.999618828f,-0.027608145f,0.998475552f,-0.0551952459f,0.996571124f,-0.0827402622f,0.993906975f,-0.110222206f,0.990485072f,-0.137620121f,
0.986308098f,-0.164913118f,0.981379211f,-0.192080393f,1.f,0.f,0.999529421f,-0.030674804f,0.998118103f,-0.061320737f,0.995767415f,-0.0919089541f,
0.992479563f,-0.122410677f,0.988257587f,-0.152797192f,0.983105481f,-0.183039889f,0.977028131f,-0.213110313f,1.f,0.f,0.999430597f,-0.0337411724f,
0.997723043f,-0.0674439222f,0.994879305f,-0.10106986f,0.990902662f,-0.134580702f,0.985797524f,-0.167938292f,0.979569793f,-0.201104641f,0.972226501f,-0.234041959f,
1.f,0.f,0.999322355f,-0.0368072242f,0.997290432f,-0.0735645667f,0.993906975f,-0.110222206f,0.989176512f,-0.146730468f,0.983105481f,-0.183039889f,
0.975702107f,-0.219101235f,0.966976464f,-0.254865646f,1.f,0.f,0.999204755f,-0.0398729257f,0.996820271f,-0.0796824396f,0.992850423f,-0.119365215f,
0.987301409f,-0.15885815f,0.980182111f,-0.198098406f,0.971503913f,-0.237023607f,0.961280465f,-0.275571823f,1.f,0.f,0.999077737f,-0.0429382585f,
0.996312618f,-0.0857973099f,0.991709769f,-0.128498107f,0.985277653f,-0.170961887f,0.977028131f,-0.213110313f,0.966976464f,-0.254865646f,0.955141187f,-0.296150893f,
1.f,0.f,0.998941302f,-0.0460031815f,0.995767415f,-0.0919089541f,0.990485072f,-0.137620121f,0.983105481f,-0.183039889f,0.973644257f,-0.228072077f,
0.962121427f,-0.272621363f,0.94856137f,-0.316593379f,1.f,0.f,0.99879545f,-0.0490676761f,0.99518472f,-0.0980171412f,0.989176512f,-0.146730468f,
0.980785251f,-0.195090324f,0.970031261f,-0.242980182f,0.956940353f,-0.290284663f,0.941544056f,-0.336889863f,1.f,0.f,0.998640239f,-0.052131705f,
0.994564593f,-0.104121633f,0.987784147f,-0.155828401f,0.97831738f,-0.207111374f,0.966189981f,-0.257831097f,0.95143503f,-0.307849646f,0.934092522f,-0.357030958f,
1.f,0.f,0.998475552f,-0.0551952459f,0.993906975f,-0.110222206f,0.986308098f,-0.164913118f,0.975702107f,-0.219101235f,0.962121427f,-0.272621363f,
0.945607305f,-0.32531029f,0.926210225f,-0.377007425f,1.f,0.f,0.998301566f,-0.0582582653f,0.993211925f,-0.116318628f,0.984748483f,-0.173983872f,
0.972939968f,-0.231058106f,0.957826436f,-0.287347466f,0.939459205f,-0.342660725f,0.917900801f,-0.396809995f,1.f,0.f,0.998118103f,-0.061320737f,
0.992479563f,-0.122410677f,0.983105481f,-0.183039889f,0.970031261f,-0.242980182f,0.953306019f,-0.302005947f,0.932992816f,-0.359895051f,0.909168005f,-0.416429549f,
1.f,0.f,0.997925282f,-0.0643826276f,0.991709769f,-0.128498107f,0.981379211f,-0.192080393f,0.966976464f,-0.254865646f,0.94856137f,-0.316593379f,
0.926210225f,-0.377007425f,0.900015891f,-0.435857087f,1.f,0.f,0.997723043f,-0.0674439222f,0.990902662f,-0.134580702f,0.979569793f,-0.201104641f,
0.963776052f,-0.266712755f,0.943593442f,-0.331106305f,0.919113874f,-0.393992037f,0.890448749f,-0.455083579f,1.f,0.f,0.997511446f,-0.070504576f,
0.990058184f,-0.140658244f,0.977677345f,-0.210111842f,0.960430503f,-0.27851969f,0.938403547f,-0.345541328f,0.91170603f,-0.410843164f,0.880470872f,-0.474100202f,
1.f,0.f,0.997290432f,-0.0735645667f,0.989176512f,-0.146730468f,0.975702107f,-0.219101235f,0.956940353f,-0.290284663f,0.932992816f,-0.359895051f,
0.903989315f,-0.427555084f,0.870086968f,-0.492898196f,1.f,0.f,0.997060061f,-0.0766238645f,0.988257587f,-0.152797192f,0.973644257f,-0.228072077f,
0.953306019f,-0.302005947f,0.927362502f,-0.374164075f,0.895966232f,-0.444122136f,0.859301805f,-0.511468828f,1.f,0.f,0.996820271f,-0.0796824396f,
0.987301409f,-0.15885815f,0.971503913f,-0.237023607f,0.949528158f,-0.313681751f,0.921514034f,-0.388345033f,0.887639642f,-0.460538715f,0.848120332f,-0.529803634f,
1.f,0.f,0.996571124f,-0.0827402622f,0.986308098f,-0.164913118f,0.969281256f,-0.24595505f,0.945607305f,-0.32531029f,0.915448725f,-0.402434647f,
0.879012227f,-0.47679922f,0.836547732f,-0.547894061f,1.f,0.f,0.996312618f,-0.0857973099f,0.985277653f,-0.170961887f,0.966976464f,-0.254865646f,
0.941544056f,-0.336889863f,0.909168005f,-0.416429549f,0.870086968f,-0.492898196f,0.824589312f,-0.565731823f,1.f,0.f,0.996044695f,-0.0888535529f,
0.984210074f,-0.177004218f,0.964589775f,-0.263754666f,0.937339008f,-0.348418683f,0.902673304f,-0.430326492f,0.860866964f,-0.50883013f,0.812250614f,-0.583308637f,
1.f,0.f,0.995767415f,-0.0919089541f,0.983105481f,-0.183039889f,0.962121427f,-0.272621363f,0.932992816f,-0.359895051f,0.895966232f,-0.444122136f,
0.851355195f,-0.524589658f,0.799537241f,-0.600616455f,1.f,0.f,0.995480776f,-0.0949634984f,0.981963873f,-0.18906866f,0.95957154f,-0.281464934f,
0.928506076f,-0.371317208f,0.889048338f,-0.457813293f,0.841554999f,-0.540171444f,0.786455214f,-0.61764729f,1.f,0.f,0.99518472f,-0.0980171412f,
0.980785251f,-0.195090324f,0.956940353f,-0.290284663f,0.923879504f,-0.382683426f,0.881921291f,-0.471396744f,0.831469595f,-0.555570245f,0.773010433f,-0.634393275f,
1.f,0.f,0.994879305f,-0.10106986f,0.979569793f,-0.201104641f,0.954228103f,-0.299079835f,0.919113874f,-0.393992037f,0.874586642f,-0.484869242f,
0.8211025f,-0.570780754f,0.759209216f,-0.65084666f,1.f,0.f,0.994564593f,-0.104121633f,0.97831738f,-0.207111374f,0.95143503f,-0.307849646f,
0.914209783f,-0.405241311f,0.867046237f,-0.498227656f,0.81045717f,-0.585797846f,0.745057762f,-0.666999936f,1.f,0.f,0.994240463f,-0.107172422f,
0.977028131f,-0.213110313f,0.94856137f,-0.316593379f,0.909168005f,-0.416429549f,0.859301805f,-0.511468828f,0.799537241f,-0.600616455f,0.730562747f,-0.682845533f,
1.f,0.f,0.993906975f,-0.110222206f,0.975702107f,-0.219101235f,0.945607305f,-0.32531029f,0.903989315f,-0.427555084f,0.851355195f,-0.524589658f,
0.78834641f,-0.615231574f,0.715730846f,-0.698376238f,1.f,0.f,0.993564129f,-0.113270953f,0.974339366f,-0.225083917f,0.94257319f,-0.333999664f,
0.898674488f,-0.438616246f,0.843208253f,-0.537587047f,0.77688849f,-0.629638255f,0.700568795f,-0.71358484f,1.f,0.f,0.993211925f,-0.116318628f,
0.972939968f,-0.231058106f,0.939459205f,-0.342660725f,0.893224299f,-0.449611336f,0.834862888f,-0.550457954f,0.765167236f,-0.643831551f,0.685083687f,-0.728464365f,
1.f,0.f,0.992850423f,-0.119365215f,0.971503913f,-0.237023607f,0.936265647f,-0.351292759f,0.887639642f,-0.460538715f,0.826321065f,-0.563199341f,
0.753186822f,-0.657806695f,0.669282615f,-0.743007958f,1.f,0.f,0.992479563f,-0.122410677f,0.970031261f,-0.242980182f,0.932992816f,-0.359895051f,
0.881921291f,-0.471396744f,0.817584813f,-0.575808167f,0.740951121f,-0.671558976f,0.653172851f,-0.757208824f,1.f,0.f,0.992099285f,-0.125454977f,
0.968522072f,-0.248927608f,0.929640889f,-0.368466824f,0.876070082f,-0.482183784f,0.808656156f,-0.588281572f,0.728464365f,-0.685083687f,0.636761844f,-0.771060526f,
1.f,0.f,0.991709769f,-0.128498107f,0.966976464f,-0.254865646f,0.926210225f,-0.377007425f,0.870086968f,-0.492898196f,0.799537241f,-0.600616455f,
0.715730846f,-0.698376238f,0.620057225f,-0.784556568f,1.f,0.f,0.991310835f,-0.13154003f,0.965394437f,-0.260794103f,0.92270112f,-0.385516047f,
0.863972843f,-0.50353837f,0.790230215f,-0.612810075f,0.702754736f,-0.711432219f,0.603066623f,-0.797690868f,1.f,0.f,0.990902662f,-0.134580702f,
0.963776052f,-0.266712755f,0.919113874f,-0.393992037f,0.857728601f,-0.514102757f,0.780737221f,-0.624859512f,0.689540565f,-0.724247098f,0.585797846f,-0.81045717f,
1.f,0.f,0.990485072f,-0.137620121f,0.962121427f,-0.272621363f,0.915448725f,-0.402434647f,0.851355195f,-0.524589658f,0.771060526f,-0.636761844f,
0.676092684f,-0.736816585f,0.568258941f,-0.82284981f,1.f,0.f,0.990058184f,-0.140658244f,0.960430503f,-0.27851969f,0.91170603f,-0.410843164f,
0.84485358f,-0.534997642f,0.761202395f,-0.64851439f,0.662415802f,-0.749136388f,0.550457954f,-0.834862888f,1.f,0.f,0.989621997f,-0.143695027f,
0.958703458f,-0.284407526f,0.907886088f,-0.419216901f,0.838224709f,-0.545324981f,0.751165152f,-0.660114348f,0.64851439f,-0.761202395f,0.532403111f,-0.84649092f,
1.f,0.f,0.989176512f,-0.146730468f,0.956940353f,-0.290284663f,0.903989315f,-0.427555084f,0.831469595f,-0.555570245f,0.740951121f,-0.671558976f,
0.634393275f,-0.773010433f,0.514102757f,-0.857728601f,1.f,0.f,0.988721669f,-0.149764538f,0.955141187f,-0.296150893f,0.900015891f,-0.435857087f,
0.824589312f,-0.565731823f,0.730562747f,-0.682845533f,0.620057225f,-0.784556568f,0.495565265f,-0.868570685f,1.f,0.f,0.988257587f,-0.152797192f,
0.953306019f,-0.302005947f,0.895966232f,-0.444122136f,0.817584813f,-0.575808167f,0.720002532f,-0.693971455f,0.605511069f,-0.795836926f,0.47679922f,-0.879012227f,
1.f,0.f,0.987784147f,-0.155828401f,0.95143503f,-0.307849646f,0.891840696f,-0.452349573f,0.81045717f,-0.585797846f,0.709272802f,-0.704934061f,
0.590759695f,-0.806847572f,0.457813293f,-0.889048338f,1.f,0.f,0.987301409f,-0.15885815f,0.949528158f,-0.313681751f,0.887639642f,-0.460538715f,
0.803207517f,-0.59569931f,0.698376238f,-0.715730846f,0.575808167f,-0.817584813f,0.438616246f,-0.898674488f,1.f,0.f,0.986809373f,-0.161886394f,
0.947585583f,-0.319502026f,0.883363366f,-0.468688816f,0.795836926f,-0.605511069f,0.687315345f,-0.726359129f,0.560661554f,-0.82804507f,0.419216901f,-0.907886088f,
1.f,0.f,0.986308098f,-0.164913118f,0.945607305f,-0.32531029f,0.879012227f,-0.47679922f,0.78834641f,-0.615231574f,0.676092684f,-0.736816585f,
0.545324981f,-0.838224709f,0.399624199f,-0.916679084f,1.f,0.f,0.985797524f,-0.167938292f,0.943593442f,-0.331106305f,0.874586642f,-0.484869242f,
0.780737221f,-0.624859512f,0.664710999f,-0.747100592f,0.529803634f,-0.848120332f,0.379847199f,-0.925049245f,1.f,0.f,0.985277653f,-0.170961887f,
0.941544056f,-0.336889863f,0.870086968f,-0.492898196f,0.773010433f,-0.634393275f,0.653172851f,-0.757208824f,0.514102757f,-0.857728601f,0.359895051f,-0.932992816f,
1.f,0.f,0.984748483f,-0.173983872f,0.939459205f,-0.342660725f,0.865513623f,-0.500885367f,0.765167236f,-0.643831551f,0.641481042f,-0.767138898f,
0.498227656f,-0.867046237f,0.339776874f,-0.940506041f,1.f,0.f,0.984210074f,-0.177004218f,0.937339008f,-0.348418683f,0.860866964f,-0.50883013f,
0.757208824f,-0.653172851f,0.629638255f,-0.77688849f,0.482183784f,-0.876070082f,0.319502026f,-0.947585583f,1.f,0.f,0.983662426f,-0.180022895f,
0.935183525f,-0.354163527f,0.856147349f,-0.516731799f,0.749136388f,-0.662415802f,0.61764729f,-0.786455214f,0.465976506f,-0.884797096f,0.299079835f,-0.954228103f,
1.f,0.f,0.983105481f,-0.183039889f,0.932992816f,-0.359895051f,0.851355195f,-0.524589658f,0.740951121f,-0.671558976f,0.605511069f,-0.795836926f,
0.449611336f,-0.893224299f,0.27851969f,-0.960430503f,1.f,0.f,0.982539296f,-0.186055154f,0.93076694f,-0.365612984f,0.84649092f,-0.532403111f,
0.732654274f,-0.680601001f,0.593232274f,-0.805031359f,0.433093816f,-0.901348829f,0.257831097f,-0.966189981f,1.f,0.f,0.981963873f,-0.18906866f,
0.928506076f,-0.371317208f,0.841554999f,-0.540171444f,0.724247098f,-0.689540565f,0.580813944f,-0.81403631f,0.416429549f,-0.909168005f,0.237023607f,-0.971503913f,
1.f,0.f,0.981379211f,-0.192080393f,0.926210225f,-0.377007425f,0.836547732f,-0.547894061f,0.715730846f,-0.698376238f,0.568258941f,-0.82284981f,
0.399624199f,-0.916679084f,0.216106802f,-0.976369739f,1.f,0.f,0.980785251f,-0.195090324f,0.923879504f,-0.382683426f,0.831469595f,-0.555570245f,
0.707106769f,-0.707106769f,0.555570245f,-0.831469595f,0.382683426f,-0.923879504f,0.195090324f,-0.980785251f,1.f,0.f,0.980182111f,-0.198098406f,
0.921514034f,-0.388345033f,0.826321065f,-0.563199341f,0.698376238f,-0.715730846f,0.542750776f,-0.839893818f,0.365612984f,-0.93076694f,0.173983872f,-0.984748483f,
1.f,0.f,0.979569793f,-0.201104641f,0.919113874f,-0.393992037f,0.8211025f,-0.570780754f,0.689540565f,-0.724247098f,0.529803634f,-0.848120332f,
0.348418683f,-0.937339008f,0.152797192f,-0.988257587f,1.f,0.f,0.978948176f,-0.204108968f,0.916679084f,-0.399624199f,0.815814435f,-0.578313768f,
0.680601001f,-0.732654274f,0.516731799f,-0.856147349f,0.331106305f,-0.943593442f,0.13154003f,-0.991310835f,1.f,0.f,0.97831738f,-0.207111374f,
0.914209783f,-0.405241311f,0.81045717f,-0.585797846f,0.671558976f,-0.740951121f,0.50353837f,-0.863972843f,0.313681751f,-0.949528158f,0.110222206f,-0.993906975f,
1.f,0.f,0.977677345f,-0.210111842f,0.91170603f,-0.410843164f,0.805031359f,-0.593232274f,0.662415802f,-0.749136388f,0.490226477f,-0.871595085f,
0.296150893f,-0.955141187f,0.0888535529f,-0.996044695f,1.f,0.f,0.977028131f,-0.213110313f,0.909168005f,-0.416429549f,0.799537241f,-0.600616455f,
0.653172851f,-0.757208824f,0.47679922f,-0.879012227f,0.27851969f,-0.960430503f,0.0674439222f,-0.997723043f,1.f,0.f,0.976369739f,-0.216106802f,
0.906595707f,-0.422000259f,0.793975472f,-0.607949793f,0.643831551f,-0.765167236f,0.463259786f,-0.886222541f,0.260794103f,-0.965394437f,0.0460031815f,-0.998941302f,
1.f,0.f,0.975702107f,-0.219101235f,0.903989315f,-0.427555084f,0.78834641f,-0.615231574f,0.634393275f,-0.773010433f,0.449611336f,-0.893224299f,
0.242980182f,-0.970031261f,0.024541229f,-0.999698818f,1.f,0.f,0.975025356f,-0.222093627f,0.901348829f,-0.433093816f,0.78265059f,-0.622461259f,
0.624859512f,-0.780737221f,0.435857087f,-0.900015891f,0.225083917f,-0.974339366f,0.00306795677f,-0.999995291f,1.f,0.f,0.974339366f,-0.225083917f,
0.898674488f,-0.438616246f,0.77688849f,-0.629638255f,0.615231574f,-0.78834641f,0.422000259f,-0.906595707f,0.207111374f,-0.97831738f,-0.0184067301f,-0.999830604f,
1.f,0.f,0.973644257f,-0.228072077f,0.895966232f,-0.444122136f,0.771060526f,-0.636761844f,0.605511069f,-0.795836926f,0.408044159f,-0.912962198f,
0.18906866f,-0.981963873f,-0.0398729257f,-0.999204755f,1.f,0.f,0.972939968f,-0.231058106f,0.893224299f,-0.449611336f,0.765167236f,-0.643831551f,
0.59569931f,-0.803207517f,0.393992037f,-0.919113874f,0.170961887f,-0.985277653f,-0.061320737f,-0.998118103f,1.f,0.f,0.972226501f,-0.234041959f,
0.890448749f,-0.455083579f,0.759209216f,-0.65084666f,0.585797846f,-0.81045717f,0.379847199f,-0.925049245f,0.152797192f,-0.988257587f,-0.0827402622f,-0.996571124f,
1.f,0.f,0.971503913f,-0.237023607f,0.887639642f,-0.460538715f,0.753186822f,-0.657806695f,0.575808167f,-0.817584813f,0.365612984f,-0.93076694f,
0.134580702f,-0.990902662f,-0.104121633f,-0.994564593f,1.f,0.f,0.970772147f,-0.24000302f,0.884797096f,-0.465976506f,0.747100592f,-0.664710999f,
0.565731823f,-0.824589312f,0.351292759f,-0.936265647f,0.116318628f,-0.993211925f,-0.125454977f,-0.992099285f,1.f,0.f,0.970031261f,-0.242980182f,
0.881921291f,-0.471396744f,0.740951121f,-0.671558976f,0.555570245f,-0.831469595f,0.336889863f,-0.941544056f,0.0980171412f,-0.99518472f,-0.146730468f,-0.989176512f,
1.f,0.f,0.969281256f,-0.24595505f,0.879012227f,-0.47679922f,0.734738886f,-0.678350031f,0.545324981f,-0.838224709f,0.322407693f,-0.946600914f,
0.0796824396f,-0.996820271f,-0.167938292f,-0.985797524f,1.f,0.f,0.968522072f,-0.248927608f,0.876070082f,-0.482183784f,0.728464365f,-0.685083687f,
0.534997642f,-0.84485358f,0.307849646f,-0.95143503f,0.061320737f,-0.998118103f,-0.18906866f,-0.981963873f,1.f,0.f,0.967753828f,-0.251897812f,
0.873094976f,-0.487550169f,0.722128212f,-0.691759229f,0.524589658f,-0.851355195f,0.293219149f,-0.95604527f,0.0429382585f,-0.999077737f,-0.210111842f,-0.977677345f,
1.f,0.f,0.966976464f,-0.254865646f,0.870086968f,-0.492898196f,0.715730846f,-0.698376238f,0.514102757f,-0.857728601f,0.27851969f,-0.960430503f,
0.024541229f,-0.999698818f,-0.231058106f,-0.972939968f,1.f,0.f,0.966189981f,-0.257831097f,0.867046237f,-0.498227656f,0.709272802f,-0.704934061f,
0.50353837f,-0.863972843f,0.263754666f,-0.964589775f,0.00613588467f,-0.999981165f,-0.251897812f,-0.967753828f,1.f,0.f,0.965394437f,-0.260794103f,
0.863972843f,-0.50353837f,0.702754736f,-0.711432219f,0.492898196f,-0.870086968f,0.248927608f,-0.968522072f,-0.0122715384f,-0.999924719f,-0.272621363f,-0.962121427f,
1.f,0.f,0.964589775f,-0.263754666f,0.860866964f,-0.50883013f,0.696177125f,-0.717870057f,0.482183784f,-0.876070082f,0.234041959f,-0.972226501f,
-0.030674804f,-0.999529421f,-0.293219149f,-0.95604527f,1.f,0.f,0.963776052f,-0.266712755f,0.857728601f,-0.514102757f,0.689540565f,-0.724247098f,
0.471396744f,-0.881921291f,0.219101235f,-0.975702107f,-0.0490676761f,-0.99879545f,-0.313681751f,-0.949528158f,1.f,0.f,0.962953269f,-0.269668311f,
0.854557991f,-0.519356012f,0.682845533f,-0.730562747f,0.460538715f,-0.887639642f,0.204108968f,-0.978948176f,-0.0674439222f,-0.997723043f,-0.333999664f,-0.94257319f,
1.f,0.f,0.962121427f,-0.272621363f,0.851355195f,-0.524589658f,0.676092684f,-0.736816585f,0.449611336f,-0.893224299f,0.18906866f,-0.981963873f,
-0.0857973099f,-0.996312618f,-0.354163527f,-0.935183525f,1.f,0.f,0.961280465f,-0.275571823f,0.848120332f,-0.529803634f,0.669282615f,-0.743007958f,
0.438616246f,-0.898674488f,0.173983872f,-0.984748483f,-0.104121633f,-0.994564593f,-0.374164075f,-0.927362502f,1.f,0.f,0.960430503f,-0.27851969f,
0.84485358f,-0.534997642f,0.662415802f,-0.749136388f,0.427555084f,-0.903989315f,0.15885815f,-0.987301409f,-0.122410677f,-0.992479563f,-0.393992037f,-0.919113874f,
1.f,0.f,0.95957154f,-0.281464934f,0.841554999f,-0.540171444f,0.655492842f,-0.755201399f,0.416429549f,-0.909168005f,0.143695027f,-0.989621997f,
-0.140658244f,-0.990058184f,-0.413638324f,-0.910441279f,1.f,0.f,0.958703458f,-0.284407526f,0.838224709f,-0.545324981f,0.64851439f,-0.761202395f,
0.405241311f,-0.914209783f,0.128498107f,-0.991709769f,-0.15885815f,-0.987301409f,-0.433093816f,-0.901348829f,1.f,0.f,0.957826436f,-0.287347466f,
0.834862888f,-0.550457954f,0.641481042f,-0.767138898f,0.393992037f,-0.919113874f,0.113270953f,-0.993564129f,-0.177004218f,-0.984210074f,-0.452349573f,-0.891840696f,
1.f,0.f,0.956940353f,-0.290284663f,0.831469595f,-0.555570245f,0.634393275f,-0.773010433f,0.382683426f,-0.923879504f,0.0980171412f,-0.99518472f,
-0.195090324f,-0.980785251f,-0.471396744f,-0.881921291f,1.f,0.f,0.95604527f,-0.293219149f,0.82804507f,-0.560661554f,0.627251804f,-0.778816521f,
0.371317208f,-0.928506076f,0.0827402622f,-0.996571124f,-0.213110313f,-0.977028131f,-0.490226477f,-0.871595085f,1.f,0.f,0.955141187f,-0.296150893f,
0.824589312f,-0.565731823f,0.620057225f,-0.784556568f,0.359895051f,-0.932992816f,0.0674439222f,-0.997723043f,-0.231058106f,-0.972939968f,-0.50883013f,-0.860866964f,
1.f,0.f,0.954228103f,-0.299079835f,0.8211025f,-0.570780754f,0.612810075f,-0.790230215f,0.348418683f,-0.937339008f,0.052131705f,-0.998640239f,
-0.248927608f,-0.968522072f,-0.527199149f,-0.849741757f,1.f,0.f,0.953306019f,-0.302005947f,0.817584813f,-0.575808167f,0.605511069f,-0.795836926f,
0.336889863f,-0.941544056f,0.0368072242f,-0.999322355f,-0.266712755f,-0.963776052f,-0.545324981f,-0.838224709f,1.f,0.f,0.952374995f,-0.304929227f,
0.81403631f,-0.580813944f,0.598160684f,-0.801376164f,0.32531029f,-0.945607305f,0.0214740802f,-0.99976939f,-0.284407526f,-0.958703458f,-0.563199341f,-0.826321065f,
1.f,0.f,0.95143503f,-0.307849646f,0.81045717f,-0.585797846f,0.590759695f,-0.806847572f,0.313681751f,-0.949528158f,0.00613588467f,-0.999981165f,
-0.302005947f,-0.953306019f,-0.580813944f,-0.81403631f,1.f,0.f,0.950486064f,-0.310767144f,0.806847572f,-0.590759695f,0.583308637f,-0.812250614f,
0.302005947f,-0.953306019f,-0.00920375437f,-0.999957621f,-0.319502026f,-0.947585583f,-0.598160684f,-0.801376164f,1.f,0.f,0.949528158f,-0.313681751f,
0.803207517f,-0.59569931f,0.575808167f,-0.817584813f,0.290284663f,-0.956940353f,-0.024541229f,-0.999698818f,-0.336889863f,-0.941544056f,-0.615231574f,-0.78834641f,
1.f,0.f,0.94856137f,-0.316593379f,0.799537241f,-0.600616455f,0.568258941f,-0.82284981f,0.27851969f,-0.960430503f,-0.0398729257f,-0.999204755f,
-0.354163527f,-0.935183525f,-0.632018745f,-0.774953127f,1.f,0.f,0.947585583f,-0.319502026f,0.795836926f,-0.605511069f,0.560661554f,-0.82804507f,
0.266712755f,-0.963776052f,-0.0551952459f,-0.998475552f,-0.371317208f,-0.928506076f,-0.64851439f,-0.761202395f,1.f,0.f,0.946600914f,-0.322407693f,
0.792106569f,-0.610382795f,0.553016722f,-0.833170176f,0.254865646f,-0.966976464f,-0.070504576f,-0.997511446f,-0.388345033f,-0.921514034f,-0.664710999f,-0.747100592f,
1.f,0.f,0.945607305f,-0.32531029f,0.78834641f,-0.615231574f,0.545324981f,-0.838224709f,0.242980182f,-0.970031261f,-0.0857973099f,-0.996312618f,
-0.405241311f,-0.914209783f,-0.680601001f,-0.732654274f,1.f,0.f,0.944604814f,-0.328209847f,0.784556568f,-0.620057225f,0.537587047f,-0.843208253f,
0.231058106f,-0.972939968f,-0.10106986f,-0.994879305f,-0.422000259f,-0.906595707f,-0.696177125f,-0.717870057f,1.f,0.f,0.943593442f,-0.331106305f,
0.780737221f,-0.624859512f,0.529803634f,-0.848120332f,0.219101235f,-0.975702107f,-0.116318628f,-0.993211925f,-0.438616246f,-0.898674488f,-0.711432219f,-0.702754736f,
1.f,0.f,0.94257319f,-0.333999664f,0.77688849f,-0.629638255f,0.521975279f,-0.852960587f,0.207111374f,-0.97831738f,-0.13154003f,-0.991310835f,
-0.455083579f,-0.890448749f,-0.726359129f,-0.687315345f,1.f,0.f,0.941544056f,-0.336889863f,0.773010433f,-0.634393275f,0.514102757f,-0.857728601f,
0.195090324f,-0.980785251f,-0.146730468f,-0.989176512f,-0.471396744f,-0.881921291f,-0.740951121f,-0.671558976f,1.f,0.f,0.940506041f,-0.339776874f,
0.769103348f,-0.639124453f,0.506186664f,-0.862423956f,0.183039889f,-0.983105481f,-0.161886394f,-0.986809373f,-0.487550169f,-0.873094976f,-0.755201399f,-0.655492842f,
1.f,0.f,0.939459205f,-0.342660725f,0.765167236f,-0.643831551f,0.498227656f,-0.867046237f,0.170961887f,-0.985277653f,-0.177004218f,-0.984210074f,
-0.50353837f,-0.863972843f,-0.769103348f,-0.639124453f,1.f,0.f,0.938403547f,-0.345541328f,0.761202395f,-0.64851439f,0.490226477f,-0.871595085f,
0.15885815f,-0.987301409f,-0.192080393f,-0.981379211f,-0.519356012f,-0.854557991f,-0.78265059f,-0.622461259f,1.f,0.f,0.937339008f,-0.348418683f,
0.757208824f,-0.653172851f,0.482183784f,-0.876070082f,0.146730468f,-0.989176512f,-0.207111374f,-0.97831738f,-0.534997642f,-0.84485358f,-0.795836926f,-0.605511069f,
1.f,0.f,0.936265647f,-0.351292759f,0.753186822f,-0.657806695f,0.474100202f,-0.880470872f,0.134580702f,-0.990902662f,-0.222093627f,-0.975025356f,
-0.550457954f,-0.834862888f,-0.808656156f,-0.588281572f,1.f,0.f,0.935183525f,-0.354163527f,0.749136388f,-0.662415802f,0.465976506f,-0.884797096f,
0.122410677f,-0.992479563f,-0.237023607f,-0.971503913f,-0.565731823f,-0.824589312f,-0.8211025f,-0.570780754f,1.f,0.f,0.934092522f,-0.357030958f,
0.745057762f,-0.666999936f,0.457813293f,-0.889048338f,0.110222206f,-0.993906975f,-0.251897812f,-0.967753828f,-0.580813944f,-0.81403631f,-0.833170176f,-0.553016722f,
1.f,0.f,0.932992816f,-0.359895051f,0.740951121f,-0.671558976f,0.449611336f,-0.893224299f,0.0980171412f,-0.99518472f,-0.266712755f,-0.963776052f,
-0.59569931f,-0.803207517f,-0.84485358f,-0.534997642f,1.f,0.f,0.931884289f,-0.362755716f,0.736816585f,-0.676092684f,0.441371262f,-0.897324562f,
0.0857973099f,-0.996312618f,-0.281464934f,-0.95957154f,-0.610382795f,-0.792106569f,-0.856147349f,-0.516731799f,1.f,0.f,0.93076694f,-0.365612984f,
0.732654274f,-0.680601001f,0.433093816f,-0.901348829f,0.0735645667f,-0.997290432f,-0.296150893f,-0.955141187f,-0.624859512f,-0.780737221f,-0.867046237f,-0.498227656f,
1.f,0.f,0.929640889f,-0.368466824f,0.728464365f,-0.685083687f,0.424779683f,-0.905296743f,0.061320737f,-0.998118103f,-0.310767144f,-0.950486064f,
-0.639124453f,-0.769103348f,-0.877545297f,-0.479493767f,1.f,0.f,0.928506076f,-0.371317208f,0.724247098f,-0.689540565f,0.416429549f,-0.909168005f,
0.0490676761f,-0.99879545f,-0.32531029f,-0.945607305f,-0.653172851f,-0.757208824f,-0.887639642f,-0.460538715f,1.f,0.f,0.927362502f,-0.374164075f,
0.720002532f,-0.693971455f,0.408044159f,-0.912962198f,0.0368072242f,-0.999322355f,-0.339776874f,-0.940506041f,-0.666999936f,-0.745057762f,-0.897324562f,-0.441371262f,
1.f,0.f,0.926210225f,-0.377007425f,0.715730846f,-0.698376238f,0.399624199f,-0.916679084f,0.024541229f,-0.999698818f,-0.354163527f,-0.935183525f,
-0.680601001f,-0.732654274f,-0.906595707f,-0.422000259f,1.f,0.f,0.925049245f,-0.379847199f,0.711432219f,-0.702754736f,0.391170382f,-0.920318305f,
0.0122715384f,-0.999924719f,-0.368466824f,-0.929640889f,-0.693971455f,-0.720002532f,-0.915448725f,-0.402434647f,1.f,0.f,0.923879504f,-0.382683426f,
0.707106769f,-0.707106769f,0.382683426f,-0.923879504f,0.f,-1.f,-0.382683426f,-0.923879504f,-0.707106769f,-0.707106769f,-0.923879504f,-0.382683426f,
1.f,0.f,0.92270112f,-0.385516047f,0.702754736f,-0.711432219f,0.374164075f,-0.927362502f,-0.0122715384f,-0.999924719f,-0.396809995f,-0.917900801f,
-0.720002532f,-0.693971455f,-0.931884289f,-0.362755716f,1.f,0.f,0.921514034f,-0.388345033f,0.698376238f,-0.715730846f,0.365612984f,-0.93076694f,
-0.024541229f,-0.999698818f,-0.410843164f,-0.91170603f,-0.732654274f,-0.680601001f,-0.939459205f,-0.342660725f,1.f,0.f,0.920318305f,-0.391170382f,
0.693971455f,-0.720002532f,0.357030958f,-0.934092522f,-0.0368072242f,-0.999322355f,-0.424779683f,-0.905296743f,-0.745057762f,-0.666999936f,-0.946600914f,-0.322407693f,
1.f,0.f,0.919113874f,-0.393992037f,0.689540565f,-0.724247098f,0.348418683f,-0.937339008f,-0.0490676761f,-0.99879545f,-0.438616246f,-0.898674488f,
-0.757208824f,-0.653172851f,-0.953306019f,-0.302005947f,1.f,0.f,0.917900801f,-0.396809995f,0.685083687f,-0.728464365f,0.339776874f,-0.940506041f,
-0.061320737f,-0.998118103f,-0.452349573f,-0.891840696f,-0.769103348f,-0.639124453f,-0.95957154f,-0.281464934f,1.f,0.f,0.916679084f,-0.399624199f,
0.680601001f,-0.732654274f,0.331106305f,-0.943593442f,-0.0735645667f,-0.997290432f,-0.465976506f,-0.884797096f,-0.780737221f,-0.624859512f,-0.965394437f,-0.260794103f,
1.f,0.f,0.915448725f,-0.402434647f,0.676092684f,-0.736816585f,0.322407693f,-0.946600914f,-0.0857973099f,-0.996312618f,-0.479493767f,-0.877545297f,
-0.792106569f,-0.610382795f,-0.970772147f,-0.24000302f,1.f,0.f,0.914209783f,-0.405241311f,0.671558976f,-0.740951121f,0.313681751f,-0.949528158f,
-0.0980171412f,-0.99518472f,-0.492898196f,-0.870086968f,-0.803207517f,-0.59569931f,-0.975702107f,-0.219101235f,1.f,0.f,0.912962198f,-0.408044159f,
0.666999936f,-0.745057762f,0.304929227f,-0.952374995f,-0.110222206f,-0.993906975f,-0.506186664f,-0.862423956f,-0.81403631f,-0.580813944f,-0.980182111f,-0.198098406f,
1.f,0.f,0.91170603f,-0.410843164f,0.662415802f,-0.749136388f,0.296150893f,-0.955141187f,-0.122410677f,-0.992479563f,-0.519356012f,-0.854557991f,
-0.824589312f,-0.565731823f,-0.984210074f,-0.177004218f,1.f,0.f,0.910441279f,-0.413638324f,0.657806695f,-0.753186822f,0.287347466f,-0.957826436f,
-0.134580702f,-0.990902662f,-0.532403111f,-0.84649092f,-0.834862888f,-0.550457954f,-0.987784147f,-0.155828401f,1.f,0.f,0.909168005f,-0.416429549f,
0.653172851f,-0.757208824f,0.27851969f,-0.960430503f,-0.146730468f,-0.989176512f,-0.545324981f,-0.838224709f,-0.84485358f,-0.534997642f,-0.990902662f,-0.134580702f,
1.f,0.f,0.907886088f,-0.419216901f,0.64851439f,-0.761202395f,0.269668311f,-0.962953269f,-0.15885815f,-0.987301409f,-0.558118522f,-0.829761207f,
-0.854557991f,-0.519356012f,-0.993564129f,-0.113270953f,1.f,0.f,0.906595707f,-0.422000259f,0.643831551f,-0.765167236f,0.260794103f,-0.965394437f,
-0.170961887f,-0.985277653f,-0.570780754f,-0.8211025f,-0.863972843f,-0.50353837f,-0.995767415f,-0.0919089541f,1.f,0.f,0.905296743f,-0.424779683f,
0.639124453f,-0.769103348f,0.251897812f,-0.967753828f,-0.183039889f,-0.983105481f,-0.583308637f,-0.812250614f,-0.873094976f,-0.487550169f,-0.997511446f,-0.070504576f,
1.f,0.f,0.903989315f,-0.427555084f,0.634393275f,-0.773010433f,0.242980182f,-0.970031261f,-0.195090324f,-0.980785251f,-0.59569931f,-0.803207517f,
-0.881921291f,-0.471396744f,-0.99879545f,-0.0490676761f,1.f,0.f,0.902673304f,-0.430326492f,0.629638255f,-0.77688849f,0.234041959f,-0.972226501f,
-0.207111374f,-0.97831738f,-0.607949793f,-0.793975472f,-0.890448749f,-0.455083579f,-0.999618828f,-0.027608145f,1.f,0.f,0.901348829f,-0.433093816f,
0.624859512f,-0.780737221f,0.225083917f,-0.974339366f,-0.219101235f,-0.975702107f,-0.620057225f,-0.784556568f,-0.898674488f,-0.438616246f,-0.999981165f,-0.00613588467f,
1.f,0.f,0.900015891f,-0.435857087f,0.620057225f,-0.784556568f,0.216106802f,-0.976369739f,-0.231058106f,-0.972939968f,-0.632018745f,-0.774953127f,
-0.906595707f,-0.422000259f,-0.99988234f,0.015339206f,1.f,0.f,0.898674488f,-0.438616246f,0.615231574f,-0.78834641f,0.207111374f,-0.97831738f,
-0.242980182f,-0.970031261f,-0.643831551f,-0.765167236f,-0.914209783f,-0.405241311f,-0.999322355f,0.0368072242f,1.f,0.f,0.897324562f,-0.441371262f,
0.610382795f,-0.792106569f,0.198098406f,-0.980182111f,-0.254865646f,-0.966976464f,-0.655492842f,-0.755201399f,-0.921514034f,-0.388345033f,-0.998301566f,0.0582582653f,
1.f,0.f,0.895966232f,-0.444122136f,0.605511069f,-0.795836926f,0.18906866f,-0.981963873f,-0.266712755f,-0.963776052f,-0.666999936f,-0.745057762f,
-0.928506076f,-0.371317208f,-0.996820271f,0.0796824396f,1.f,0.f,0.894599497f,-0.446868837f,0.600616455f,-0.799537241f,0.180022895f,-0.983662426f,
-0.27851969f,-0.960430503f,-0.678350031f,-0.734738886f,-0.935183525f,-0.354163527f,-0.994879305f,0.10106986f,1.f,0.f,0.893224299f,-0.449611336f,
0.59569931f,-0.803207517f,0.170961887f,-0.985277653f,-0.290284663f,-0.956940353f,-0.689540565f,-0.724247098f,-0.941544056f,-0.336889863f,-0.992479563f,0.122410677f,
1.f,0.f,0.891840696f,-0.452349573f,0.590759695f,-0.806847572f,0.161886394f,-0.986809373f,-0.302005947f,-0.953306019f,-0.700568795f,-0.71358484f,
-0.947585583f,-0.319502026f,-0.989621997f,0.143695027f,1.f,0.f,0.890448749f,-0.455083579f,0.585797846f,-0.81045717f,0.152797192f,-0.988257587f,
-0.313681751f,-0.949528158f,-0.711432219f,-0.702754736f,-0.953306019f,-0.302005947f,-0.986308098f,0.164913118f,1.f,0.f,0.889048338f,-0.457813293f,
0.580813944f,-0.81403631f,0.143695027f,-0.989621997f,-0.32531029f,-0.945607305f,-0.722128212f,-0.691759229f,-0.958703458f,-0.284407526f,-0.982539296f,0.186055154f,
1.f,0.f,0.887639642f,-0.460538715f,0.575808167f,-0.817584813f,0.134580702f,-0.990902662f,-0.336889863f,-0.941544056f,-0.732654274f,-0.680601001f,
-0.963776052f,-0.266712755f,-0.97831738f,0.207111374f,1.f,0.f,0.886222541f,-0.463259786f,0.570780754f,-0.8211025f,0.125454977f,-0.992099285f,
-0.348418683f,-0.937339008f,-0.743007958f,-0.669282615f,-0.968522072f,-0.248927608f,-0.973644257f,0.228072077f,1.f,0.f,0.884797096f,-0.465976506f,
0.565731823f,-0.824589312f,0.116318628f,-0.993211925f,-0.359895051f,-0.932992816f,-0.753186822f,-0.657806695f,-0.972939968f,-0.231058106f,-0.968522072f,0.248927608f,
1.f,0.f,0.883363366f,-0.468688816f,0.560661554f,-0.82804507f,0.107172422f,-0.994240463f,-0.371317208f,-0.928506076f,-0.763188422f,-0.64617604f,
-0.977028131f,-0.213110313f,-0.962953269f,0.269668311f,1.f,0.f,0.881921291f,-0.471396744f,0.555570245f,-0.831469595f,0.0980171412f,-0.99518472f,
-0.382683426f,-0.923879504f,-0.773010433f,-0.634393275f,-0.980785251f,-0.195090324f,-0.956940353f,0.290284663f,1.f,0.f,0.880470872f,-0.474100202f,
0.550457954f,-0.834862888f,0.0888535529f,-0.996044695f,-0.393992037f,-0.919113874f,-0.78265059f,-0.622461259f,-0.984210074f,-0.177004218f,-0.950486064f,0.310767144f,
1.f,0.f,0.879012227f,-0.47679922f,0.545324981f,-0.838224709f,0.0796824396f,-0.996820271f,-0.405241311f,-0.914209783f,-0.792106569f,-0.610382795f,
-0.987301409f,-0.15885815f,-0.943593442f,0.331106305f,1.f,0.f,0.877545297f,-0.479493767f,0.540171444f,-0.841554999f,0.070504576f,-0.997511446f,
-0.416429549f,-0.909168005f,-0.801376164f,-0.598160684f,-0.990058184f,-0.140658244f,-0.936265647f,0.351292759f,1.f,0.f,0.876070082f,-0.482183784f,
0.534997642f,-0.84485358f,0.061320737f,-0.998118103f,-0.427555084f,-0.903989315f,-0.81045717f,-0.585797846f,-0.992479563f,-0.122410677f,-0.928506076f,0.371317208f,
1.f,0.f,0.874586642f,-0.484869242f,0.529803634f,-0.848120332f,0.052131705f,-0.998640239f,-0.438616246f,-0.898674488f,-0.819347501f,-0.573297143f,
-0.994564593f,-0.104121633f,-0.920318305f,0.391170382f,1.f,0.f,0.873094976f,-0.487550169f,0.524589658f,-0.851355195f,0.0429382585f,-0.999077737f,
-0.449611336f,-0.893224299f,-0.82804507f,-0.560661554f,-0.996312618f,-0.0857973099f,-0.91170603f,0.410843164f,1.f,0.f,0.871595085f,-0.490226477f,
0.519356012f,-0.854557991f,0.0337411724f,-0.999430597f,-0.460538715f,-0.887639642f,-0.836547732f,-0.547894061f,-0.997723043f,-0.0674439222f,-0.902673304f,0.430326492f,
1.f,0.f,0.870086968f,-0.492898196f,0.514102757f,-0.857728601f,0.024541229f,-0.999698818f,-0.471396744f,-0.881921291f,-0.84485358f,-0.534997642f,
-0.99879545f,-0.0490676761f,-0.893224299f,0.449611336f,1.f,0.f,0.868570685f,-0.495565265f,0.50883013f,-0.860866964f,0.015339206f,-0.99988234f,
-0.482183784f,-0.876070082f,-0.852960587f,-0.521975279f,-0.999529421f,-0.030674804f,-0.883363366f,0.468688816f,1.f,0.f,0.867046237f,-0.498227656f,
0.50353837f,-0.863972843f,0.00613588467f,-0.999981165f,-0.492898196f,-0.870086968f,-0.860866964f,-0.50883013f,-0.999924719f,-0.0122715384f,-0.873094976f,0.487550169f,
1.f,0.f,0.865513623f,-0.500885367f,0.498227656f,-0.867046237f,-0.00306795677f,-0.999995291f,-0.50353837f,-0.863972843f,-0.868570685f,-0.495565265f,
-0.999981165f,0.00613588467f,-0.862423956f,0.506186664f,1.f,0.f,0.863972843f,-0.50353837f,0.492898196f,-0.870086968f,-0.0122715384f,-0.999924719f,
-0.514102757f,-0.857728601f,-0.876070082f,-0.482183784f,-0.999698818f,0.024541229f,-0.851355195f,0.524589658f,1.f,0.f,0.862423956f,-0.506186664f,
0.487550169f,-0.873094976f,-0.0214740802f,-0.99976939f,-0.524589658f,-0.851355195f,-0.883363366f,-0.468688816f,-0.999077737f,0.0429382585f,-0.839893818f,0.542750776f,
1.f,0.f,0.860866964f,-0.50883013f,0.482183784f,-0.876070082f,-0.030674804f,-0.999529421f,-0.534997642f,-0.84485358f,-0.890448749f,-0.455083579f,
-0.998118103f,0.061320737f,-0.82804507f,0.560661554f,1.f,0.f,0.859301805f,-0.511468828f,0.47679922f,-0.879012227f,-0.0398729257f,-0.999204755f,
-0.545324981f,-0.838224709f,-0.897324562f,-0.441371262f,-0.996820271f,0.0796824396f,-0.815814435f,0.578313768f,1.f,0.f,0.857728601f,-0.514102757f,
0.471396744f,-0.881921291f,-0.0490676761f,-0.99879545f,-0.555570245f,-0.831469595f,-0.903989315f,-0.427555084f,-0.99518472f,0.0980171412f,-0.803207517f,0.59569931f,
1.f,0.f,0.856147349f,-0.516731799f,0.465976506f,-0.884797096f,-0.0582582653f,-0.998301566f,-0.565731823f,-0.824589312f,-0.910441279f,-0.413638324f,
-0.993211925f,0.116318628f,-0.790230215f,0.612810075f,1.f,0.f,0.854557991f,-0.519356012f,0.460538715f,-0.887639642f,-0.0674439222f,-0.997723043f,
-0.575808167f,-0.817584813f,-0.916679084f,-0.399624199f,-0.990902662f,0.134580702f,-0.77688849f,0.629638255f,1.f,0.f,0.852960587f,-0.521975279f,
0.455083579f,-0.890448749f,-0.0766238645f,-0.997060061f,-0.585797846f,-0.81045717f,-0.92270112f,-0.385516047f,-0.988257587f,0.152797192f,-0.763188422f,0.64617604f,
1.f,0.f,0.851355195f,-0.524589658f,0.449611336f,-0.893224299f,-0.0857973099f,-0.996312618f,-0.59569931f,-0.803207517f,-0.928506076f,-0.371317208f,
-0.985277653f,0.170961887f,-0.749136388f,0.662415802f,1.f,0.f,0.849741757f,-0.527199149f,0.444122136f,-0.895966232f,-0.0949634984f,-0.995480776f,
-0.605511069f,-0.795836926f,-0.934092522f,-0.357030958f,-0.981963873f,0.18906866f,-0.734738886f,0.678350031f,1.f,0.f,0.848120332f,-0.529803634f,
0.438616246f,-0.898674488f,-0.104121633f,-0.994564593f,-0.615231574f,-0.78834641f,-0.939459205f,-0.342660725f,-0.97831738f,0.207111374f,-0.720002532f,0.693971455f,
1.f,0.f,0.84649092f,-0.532403111f,0.433093816f,-0.901348829f,-0.113270953f,-0.993564129f,-0.624859512f,-0.780737221f,-0.944604814f,-0.328209847f,
-0.974339366f,0.225083917f,-0.704934061f,0.709272802f,1.f,0.f,0.84485358f,-0.534997642f,0.427555084f,-0.903989315f,-0.122410677f,-0.992479563f,
-0.634393275f,-0.773010433f,-0.949528158f,-0.313681751f,-0.970031261f,0.242980182f,-0.689540565f,0.724247098f,1.f,0.f,0.843208253f,-0.537587047f,
0.422000259f,-0.906595707f,-0.13154003f,-0.991310835f,-0.643831551f,-0.765167236f,-0.954228103f,-0.299079835f,-0.965394437f,0.260794103f,-0.673829019f,0.73888731f,
1.f,0.f,0.841554999f,-0.540171444f,0.416429549f,-0.909168005f,-0.140658244f,-0.990058184f,-0.653172851f,-0.757208824f,-0.958703458f,-0.284407526f,
-0.960430503f,0.27851969f,-0.657806695f,0.753186822f,1.f,0.f,0.839893818f,-0.542750776f,0.410843164f,-0.91170603f,-0.149764538f,-0.988721669f,
-0.662415802f,-0.749136388f,-0.962953269f,-0.269668311f,-0.955141187f,0.296150893f,-0.641481042f,0.767138898f,1.f,0.f,0.838224709f,-0.545324981f,
0.405241311f,-0.914209783f,-0.15885815f,-0.987301409f,-0.671558976f,-0.740951121f,-0.966976464f,-0.254865646f,-0.949528158f,0.313681751f,-0.624859512f,0.780737221f,
1.f,0.f,0.836547732f,-0.547894061f,0.399624199f,-0.916679084f,-0.167938292f,-0.985797524f,-0.680601001f,-0.732654274f,-0.970772147f,-0.24000302f,
-0.943593442f,0.331106305f,-0.607949793f,0.793975472f,1.f,0.f,0.834862888f,-0.550457954f,0.393992037f,-0.919113874f,-0.177004218f,-0.984210074f,
-0.689540565f,-0.724247098f,-0.974339366f,-0.225083917f,-0.937339008f,0.348418683f,-0.590759695f,0.806847572f,1.f,0.f,0.833170176f,-0.553016722f,
0.388345033f,-0.921514034f,-0.186055154f,-0.982539296f,-0.698376238f,-0.715730846f,-0.977677345f,-0.210111842f,-0.93076694f,0.365612984f,-0.573297143f,0.819347501f,
1.f,0.f,0.831469595f,-0.555570245f,0.382683426f,-0.923879504f,-0.195090324f,-0.980785251f,-0.707106769f,-0.707106769f,-0.980785251f,-0.195090324f,
-0.923879504f,0.382683426f,-0.555570245f,0.831469595f,1.f,0.f,0.829761207f,-0.558118522f,0.377007425f,-0.926210225f,-0.204108968f,-0.978948176f,
-0.715730846f,-0.698376238f,-0.983662426f,-0.180022895f,-0.916679084f,0.399624199f,-0.537587047f,0.843208253f,1.f,0.f,0.82804507f,-0.560661554f,
0.371317208f,-0.928506076f,-0.213110313f,-0.977028131f,-0.724247098f,-0.689540565f,-0.986308098f,-0.164913118f,-0.909168005f,0.416429549f,-0.519356012f,0.854557991f,
1.f,0.f,0.826321065f,-0.563199341f,0.365612984f,-0.93076694f,-0.222093627f,-0.975025356f,-0.732654274f,-0.680601001f,-0.988721669f,-0.149764538f,
-0.901348829f,0.433093816f,-0.500885367f,0.865513623f,1.f,0.f,0.824589312f,-0.565731823f,0.359895051f,-0.932992816f,-0.231058106f,-0.972939968f,
-0.740951121f,-0.671558976f,-0.990902662f,-0.134580702f,-0.893224299f,0.449611336f,-0.482183784f,0.876070082f,1.f,0.f,0.82284981f,-0.568258941f,
0.354163527f,-0.935183525f,-0.24000302f,-0.970772147f,-0.749136388f,-0.662415802f,-0.992850423f,-0.119365215f,-0.884797096f,0.465976506f,-0.463259786f,0.886222541f,
1.f,0.f,0.8211025f,-0.570780754f,0.348418683f,-0.937339008f,-0.248927608f,-0.968522072f,-0.757208824f,-0.653172851f,-0.994564593f,-0.104121633f,
-0.876070082f,0.482183784f,-0.444122136f,0.895966232f,1.f,0.f,0.819347501f,-0.573297143f,0.342660725f,-0.939459205f,-0.257831097f,-0.966189981f,
-0.765167236f,-0.643831551f,-0.996044695f,-0.0888535529f,-0.867046237f,0.498227656f,-0.424779683f,0.905296743f,1.f,0.f,0.817584813f,-0.575808167f,
0.336889863f,-0.941544056f,-0.266712755f,-0.963776052f,-0.773010433f,-0.634393275f,-0.997290432f,-0.0735645667f,-0.857728601f,0.514102757f,-0.405241311f,0.914209783f,
1.f,0.f,0.815814435f,-0.578313768f,0.331106305f,-0.943593442f,-0.275571823f,-0.961280465f,-0.780737221f,-0.624859512f,-0.998301566f,-0.0582582653f,
-0.848120332f,0.529803634f,-0.385516047f,0.92270112f,1.f,0.f,0.81403631f,-0.580813944f,0.32531029f,-0.945607305f,-0.284407526f,-0.958703458f,
-0.78834641f,-0.615231574f,-0.999077737f,-0.0429382585f,-0.838224709f,0.545324981f,-0.365612984f,0.93076694f,1.f,0.f,0.812250614f,-0.583308637f,
0.319502026f,-0.947585583f,-0.293219149f,-0.95604527f,-0.795836926f,-0.605511069f,-0.999618828f,-0.027608145f,-0.82804507f,0.560661554f,-0.345541328f,0.938403547f,
1.f,0.f,0.81045717f,-0.585797846f,0.313681751f,-0.949528158f,-0.302005947f,-0.953306019f,-0.803207517f,-0.59569931f,-0.999924719f,-0.0122715384f,
-0.817584813f,0.575808167f,-0.32531029f,0.945607305f,1.f,0.f,0.808656156f,-0.588281572f,0.307849646f,-0.95143503f,-0.310767144f,-0.950486064f,
-0.81045717f,-0.585797846f,-0.999995291f,0.00306795677f,-0.806847572f,0.590759695f,-0.304929227f,0.952374995f,1.f,0.f,0.806847572f,-0.590759695f,
0.302005947f,-0.953306019f,-0.319502026f,-0.947585583f,-0.817584813f,-0.575808167f,-0.999830604f,0.0184067301f,-0.795836926f,0.605511069f,-0.284407526f,0.958703458f,
1.f,0.f,0.805031359f,-0.593232274f,0.296150893f,-0.955141187f,-0.328209847f,-0.944604814f,-0.824589312f,-0.565731823f,-0.999430597f,0.0337411724f,
-0.784556568f,0.620057225f,-0.263754666f,0.964589775f,1.f,0.f,0.803207517f,-0.59569931f,0.290284663f,-0.956940353f,-0.336889863f,-0.941544056f,
-0.831469595f,-0.555570245f,-0.99879545f,0.0490676761f,-0.773010433f,0.634393275f,-0.242980182f,0.970031261f,1.f,0.f,0.801376164f,-0.598160684f,
0.284407526f,-0.958703458f,-0.345541328f,-0.938403547f,-0.838224709f,-0.545324981f,-0.997925282f,0.0643826276f,-0.761202395f,0.64851439f,-0.222093627f,0.975025356f,
1.f,0.f,0.799537241f,-0.600616455f,0.27851969f,-0.960430503f,-0.354163527f,-0.935183525f,-0.84485358f,-0.534997642f,-0.996820271f,0.0796824396f,
-0.749136388f,0.662415802f,-0.201104641f,0.979569793f,1.f,0.f,0.797690868f,-0.603066623f,0.272621363f,-0.962121427f,-0.362755716f,-0.931884289f,
-0.851355195f,-0.524589658f,-0.995480776f,0.0949634984f,-0.736816585f,0.676092684f,-0.180022895f,0.983662426f,1.f,0.f,0.795836926f,-0.605511069f,
0.266712755f,-0.963776052f,-0.371317208f,-0.928506076f,-0.857728601f,-0.514102757f,-0.993906975f,0.110222206f,-0.724247098f,0.689540565f,-0.15885815f,0.987301409f,
1.f,0.f,0.793975472f,-0.607949793f,0.260794103f,-0.965394437f,-0.379847199f,-0.925049245f,-0.863972843f,-0.50353837f,-0.992099285f,0.125454977f,
-0.711432219f,0.702754736f,-0.137620121f,0.990485072f,1.f,0.f,0.792106569f,-0.610382795f,0.254865646f,-0.966976464f,-0.388345033f,-0.921514034f,
-0.870086968f,-0.492898196f,-0.990058184f,0.140658244f,-0.698376238f,0.715730846f,-0.116318628f,0.993211925f,1.f,0.f,0.790230215f,-0.612810075f,
0.248927608f,-0.968522072f,-0.396809995f,-0.917900801f,-0.876070082f,-0.482183784f,-0.987784147f,0.155828401f,-0.685083687f,0.728464365f,-0.0949634984f,0.995480776f,
1.f,0.f,0.78834641f,-0.615231574f,0.242980182f,-0.970031261f,-0.405241311f,-0.914209783f,-0.881921291f,-0.471396744f,-0.985277653f,0.170961887f,
-0.671558976f,0.740951121f,-0.0735645667f,0.997290432f,1.f,0.f,0.786455214f,-0.61764729f,0.237023607f,-0.971503913f,-0.413638324f,-0.910441279f,
-0.887639642f,-0.460538715f,-0.982539296f,0.186055154f,-0.657806695f,0.753186822f,-0.052131705f,0.998640239f,1.f,0.f,0.784556568f,-0.620057225f,
0.231058106f,-0.972939968f,-0.422000259f,-0.906595707f,-0.893224299f,-0.449611336f,-0.979569793f,0.201104641f,-0.643831551f,0.765167236f,-0.030674804f,0.999529421f,
1.f,0.f,0.78265059f,-0.622461259f,0.225083917f,-0.974339366f,-0.430326492f,-0.902673304f,-0.898674488f,-0.438616246f,-0.976369739f,0.216106802f,
-0.629638255f,0.77688849f,-0.00920375437f,0.999957621f,1.f,0.f,0.780737221f,-0.624859512f,0.219101235f,-0.975702107f,-0.438616246f,-0.898674488f,
-0.903989315f,-0.427555084f,-0.972939968f,0.231058106f,-0.615231574f,0.78834641f,0.0122715384f,0.999924719f,1.f,0.f,0.778816521f,-0.627251804f,
0.213110313f,-0.977028131f,-0.446868837f,-0.894599497f,-0.909168005f,-0.416429549f,-0.969281256f,0.24595505f,-0.600616455f,0.799537241f,0.0337411724f,0.999430597f,
1.f,0.f,0.77688849f,-0.629638255f,0.207111374f,-0.97831738f,-0.455083579f,-0.890448749f,-0.914209783f,-0.405241311f,-0.965394437f,0.260794103f,
-0.585797846f,0.81045717f,0.0551952459f,0.998475552f,1.f,0.f,0.774953127f,-0.632018745f,0.201104641f,-0.979569793f,-0.463259786f,-0.886222541f,
-0.919113874f,-0.393992037f,-0.961280465f,0.275571823f,-0.570780754f,0.8211025f,0.0766238645f,0.997060061f,1.f,0.f,0.773010433f,-0.634393275f,
0.195090324f,-0.980785251f,-0.471396744f,-0.881921291f,-0.923879504f,-0.382683426f,-0.956940353f,0.290284663f,-0.555570245f,0.831469595f,0.0980171412f,0.99518472f,
1.f,0.f,0.771060526f,-0.636761844f,0.18906866f,-0.981963873f,-0.479493767f,-0.877545297f,-0.928506076f,-0.371317208f,-0.952374995f,0.304929227f,
-0.540171444f,0.841554999f,0.119365215f,0.992850423f,1.f,0.f,0.769103348f,-0.639124453f,0.183039889f,-0.983105481f,-0.487550169f,-0.873094976f,
-0.932992816f,-0.359895051f,-0.947585583f,0.319502026f,-0.524589658f,0.851355195f,0.140658244f,0.990058184f,1.f,0.f,0.767138898f,-0.641481042f,
0.177004218f,-0.984210074f,-0.495565265f,-0.868570685f,-0.937339008f,-0.348418683f,-0.94257319f,0.333999664f,-0.50883013f,0.860866964f,0.161886394f,0.986809373f,
1.f,0.f,0.765167236f,-0.643831551f,0.170961887f,-0.985277653f,-0.50353837f,-0.863972843f,-0.941544056f,-0.336889863f,-0.937339008f,0.348418683f,
-0.492898196f,0.870086968f,0.183039889f,0.983105481f,1.f,0.f,0.763188422f,-0.64617604f,0.164913118f,-0.986308098f,-0.511468828f,-0.859301805f,
-0.945607305f,-0.32531029f,-0.931884289f,0.362755716f,-0.47679922f,0.879012227f,0.204108968f,0.978948176f,1.f,0.f,0.761202395f,-0.64851439f,
0.15885815f,-0.987301409f,-0.519356012f,-0.854557991f,-0.949528158f,-0.313681751f,-0.926210225f,0.377007425f,-0.460538715f,0.887639642f,0.225083917f,0.974339366f,
1.f,0.f,0.759209216f,-0.65084666f,0.152797192f,-0.988257587f,-0.527199149f,-0.849741757f,-0.953306019f,-0.302005947f,-0.920318305f,0.391170382f,
-0.444122136f,0.895966232f,0.24595505f,0.969281256f,1.f,0.f,0.757208824f,-0.653172851f,0.146730468f,-0.989176512f,-0.534997642f,-0.84485358f,
-0.956940353f,-0.290284663f,-0.914209783f,0.405241311f,-0.427555084f,0.903989315f,0.266712755f,0.963776052f,1.f,0.f,0.755201399f,-0.655492842f,
0.140658244f,-0.990058184f,-0.542750776f,-0.839893818f,-0.960430503f,-0.27851969f,-0.907886088f,0.419216901f,-0.410843164f,0.91170603f,0.287347466f,0.957826436f,
1.f,0.f,0.753186822f,-0.657806695f,0.134580702f,-0.990902662f,-0.550457954f,-0.834862888f,-0.963776052f,-0.266712755f,-0.901348829f,0.433093816f,
-0.393992037f,0.919113874f,0.307849646f,0.95143503f,1.f,0.f,0.751165152f,-0.660114348f,0.128498107f,-0.991709769f,-0.558118522f,-0.829761207f,
-0.966976464f,-0.254865646f,-0.894599497f,0.446868837f,-0.377007425f,0.926210225f,0.328209847f,0.944604814f,1.f,0.f,0.749136388f,-0.662415802f,
0.122410677f,-0.992479563f,-0.565731823f,-0.824589312f,-0.970031261f,-0.242980182f,-0.887639642f,0.460538715f,-0.359895051f,0.932992816f,0.348418683f,0.937339008f,
1.f,0.f,0.747100592f,-0.664710999f,0.116318628f,-0.993211925f,-0.573297143f,-0.819347501f,-0.972939968f,-0.231058106f,-0.880470872f,0.474100202f,
-0.342660725f,0.939459205f,0.368466824f,0.929640889f,1.f,0.f,0.745057762f,-0.666999936f,0.110222206f,-0.993906975f,-0.580813944f,-0.81403631f,
-0.975702107f,-0.219101235f,-0.873094976f,0.487550169f,-0.32531029f,0.945607305f,0.388345033f,0.921514034f,1.f,0.f,0.743007958f,-0.669282615f,
0.104121633f,-0.994564593f,-0.588281572f,-0.808656156f,-0.97831738f,-0.207111374f,-0.865513623f,0.500885367f,-0.307849646f,0.95143503f,0.408044159f,0.912962198f,
1.f,0.f,0.740951121f,-0.671558976f,0.0980171412f,-0.99518472f,-0.59569931f,-0.803207517f,-0.980785251f,-0.195090324f,-0.857728601f,0.514102757f,
-0.290284663f,0.956940353f,0.427555084f,0.903989315f,1.f,0.f,0.73888731f,-0.673829019f,0.0919089541f,-0.995767415f,-0.603066623f,-0.797690868f,
-0.983105481f,-0.183039889f,-0.849741757f,0.527199149f,-0.272621363f,0.962121427f,0.446868837f,0.894599497f,1.f,0.f,0.736816585f,-0.676092684f,
0.0857973099f,-0.996312618f,-0.610382795f,-0.792106569f,-0.985277653f,-0.170961887f,-0.841554999f,0.540171444f,-0.254865646f,0.966976464f,0.465976506f,0.884797096f,
1.f,0.f,0.734738886f,-0.678350031f,0.0796824396f,-0.996820271f,-0.61764729f,-0.786455214f,-0.987301409f,-0.15885815f,-0.833170176f,0.553016722f,
-0.237023607f,0.971503913f,0.484869242f,0.874586642f,1.f,0.f,0.732654274f,-0.680601001f,0.0735645667f,-0.997290432f,-0.624859512f,-0.780737221f,
-0.989176512f,-0.146730468f,-0.824589312f,0.565731823f,-0.219101235f,0.975702107f,0.50353837f,0.863972843f,1.f,0.f,0.730562747f,-0.682845533f,
0.0674439222f,-0.997723043f,-0.632018745f,-0.774953127f,-0.990902662f,-0.134580702f,-0.815814435f,0.578313768f,-0.201104641f,0.979569793f,0.521975279f,0.852960587f,
1.f,0.f,0.728464365f,-0.685083687f,0.061320737f,-0.998118103f,-0.639124453f,-0.769103348f,-0.992479563f,-0.122410677f,-0.806847572f,0.590759695f,
-0.183039889f,0.983105481f,0.540171444f,0.841554999f,1.f,0.f,0.726359129f,-0.687315345f,0.0551952459f,-0.998475552f,-0.64617604f,-0.763188422f,
-0.993906975f,-0.110222206f,-0.797690868f,0.603066623f,-0.164913118f,0.986308098f,0.558118522f,0.829761207f,1.f,0.f,0.724247098f,-0.689540565f,
0.0490676761f,-0.99879545f,-0.653172851f,-0.757208824f,-0.99518472f,-0.0980171412f,-0.78834641f,0.615231574f,-0.146730468f,0.989176512f,0.575808167f,0.817584813f,
1.f,0.f,0.722128212f,-0.691759229f,0.0429382585f,-0.999077737f,-0.660114348f,-0.751165152f,-0.996312618f,-0.0857973099f,-0.778816521f,0.627251804f,
-0.128498107f,0.991709769f,0.593232274f,0.805031359f,1.f,0.f,0.720002532f,-0.693971455f,0.0368072242f,-0.999322355f,-0.666999936f,-0.745057762f,
-0.997290432f,-0.0735645667f,-0.769103348f,0.639124453f,-0.110222206f,0.993906975f,0.610382795f,0.792106569f,1.f,0.f,0.717870057f,-0.696177125f,
0.030674804f,-0.999529421f,-0.673829019f,-0.73888731f,-0.998118103f,-0.061320737f,-0.759209216f,0.65084666f,-0.0919089541f,0.995767415f,0.627251804f,0.778816521f,
1.f,0.f,0.715730846f,-0.698376238f,0.024541229f,-0.999698818f,-0.680601001f,-0.732654274f,-0.99879545f,-0.0490676761f,-0.749136388f,0.662415802f,
-0.0735645667f,0.997290432f,0.643831551f,0.765167236f,1.f,0.f,0.71358484f,-0.700568795f,0.0184067301f,-0.999830604f,-0.687315345f,-0.726359129f,
-0.999322355f,-0.0368072242f,-0.73888731f,0.673829019f,-0.0551952459f,0.998475552f,0.660114348f,0.751165152f,1.f,0.f,0.711432219f,-0.702754736f,
0.0122715384f,-0.999924719f,-0.693971455f,-0.720002532f,-0.999698818f,-0.024541229f,-0.728464365f,0.685083687f,-0.0368072242f,0.999322355f,0.676092684f,0.736816585f,
1.f,0.f,0.709272802f,-0.704934061f,0.00613588467f,-0.999981165f,-0.700568795f,-0.71358484f,-0.999924719f,-0.0122715384f,-0.717870057f,0.696177125f,
-0.0184067301f,0.999830604f,0.691759229f,0.722128212f
};

#define PFON 1
#define WSL 0
#define CODEPF 1
#define NTST 1
template <class T> __device__ __forceinline__ void st_wt(T* p, T v);
#define ST(p, v) st_wt((p), (v))
#define KS 304
#define NPART 144

__device__ __forceinline__ u32 ld32(const u32* p) { return __hip_atomic_load(p, __ATOMIC_RELAXED, __HIP_MEMORY_SCOPE_WAVEFRONT); }
__device__ __forceinline__ u32 ld16(const u16* p) { return (u32)__hip_atomic_load(p, __ATOMIC_RELAXED, __HIP_MEMORY_SCOPE_WAVEFRONT); }
__device__ __forceinline__ f2 ld64(const f2* p) {
    unsigned long long v = __hip_atomic_load(reinterpret_cast<const unsigned long long*>(p), __ATOMIC_RELAXED, __HIP_MEMORY_SCOPE_WAVEFRONT);
    return __builtin_bit_cast(f2, v);
}
__device__ __forceinline__ f2 twF(int b, int p)  { return reinterpret_cast<const f2*>(g_twF)[b * 8 + p]; }
#define TAB_A 0
#define TAB_B 324
#define TAB_5 356

template <> __device__ __forceinline__ void st_wt<u32>(u32* p, u32 v) { __hip_atomic_store(p, v, __ATOMIC_RELAXED, __HIP_MEMORY_SCOPE_AGENT); }
template <> __device__ __forceinline__ void st_wt<f2>(f2* p, f2 v) { __hip_atomic_store(reinterpret_cast<unsigned long long*>(p), __builtin_bit_cast(unsigned long long, v), __ATOMIC_RELAXED, __HIP_MEMORY_SCOPE_AGENT); }
__device__ __forceinline__ float rbf(float x) { return (float)(__bf16)x; }
__device__ __forceinline__ u32 bfbits(float x) { return (u32)__builtin_bit_cast(u16, (__bf16)x); }
__device__ __forceinline__ u32 pack2(f2 v) { u32 r; asm("v_cvt_pk_bf16_f32 %0, %1, %2" : "=v"(r) : "v"(v.x), "v"(v.y)); return r; }
__device__ __forceinline__ float lo16f(u32 v) { return __builtin_bit_cast(float, v << 16); }
__device__ __forceinline__ float hi16f(u32 v) { return __builtin_bit_cast(float, v & 0xffff0000u); }
__device__ __forceinline__ f2 unpack2(u32 v) { return f2{lo16f(v), hi16f(v)}; }
__device__ __forceinline__ f2 rbf2(f2 v) { return unpack2(pack2(v)); }

__device__ __forceinline__ f2 add_mi(f2 a, f2 b) { f2 r; asm("v_pk_add_f32 %0, %1, %2 op_sel:[0,1] op_sel_hi:[1,0] neg_hi:[0,1]" : "=v"(r) : "v"(a), "v"(b)); return r; }
__device__ __forceinline__ f2 add_pi(f2 a, f2 b) { f2 r; asm("v_pk_add_f32 %0, %1, %2 op_sel:[0,1] op_sel_hi:[1,0] neg_lo:[0,1]" : "=v"(r) : "v"(a), "v"(b)); return r; }
__device__ __forceinline__ f2 add_cj(f2 a, f2 b) { f2 r; asm("v_pk_add_f32 %0, %1, %2 neg_hi:[0,1]" : "=v"(r) : "v"(a), "v"(b)); return r; }
__device__ __forceinline__ f2 sub_cj(f2 a, f2 b) { f2 r; asm("v_pk_add_f32 %0, %1, %2 neg_lo:[0,1]" : "=v"(r) : "v"(a), "v"(b)); return r; }
__device__ __forceinline__ f2 cmul(f2 a, f2 w) {
    f2 t, r;
    asm("v_pk_mul_f32 %0, %1, %2 op_sel:[0,0] op_sel_hi:[1,0]" : "=v"(t) : "v"(a), "v"(w));
    asm("v_pk_fma_f32 %0, %1, %2, %3 op_sel:[1,1,0] op_sel_hi:[0,1,1] neg_lo:[1,0,0]" : "=v"(r) : "v"(a), "v"(w), "v"(t));
    return r;
}
__device__ __forceinline__ f2 cmulc(f2 a, f2 w) {
    f2 t, r;
    asm("v_pk_mul_f32 %0, %1, %2 op_sel:[0,0] op_sel_hi:[1,0]" : "=v"(t) : "v"(a), "v"(w));
    asm("v_pk_fma_f32 %0, %1, %2, %3 op_sel:[1,1,0] op_sel_hi:[0,1,1] neg_hi:[1,0,0]" : "=v"(r) : "v"(a), "v"(w), "v"(t));
    return r;
}
__device__ __forceinline__ f2 cmac(f2 c, f2 a, f2 w) {
    f2 t, r;
    asm("v_pk_fma_f32 %0, %1, %2, %3 op_sel:[0,0,0] op_sel_hi:[1,0,1]" : "=v"(t) : "v"(a), "v"(w), "v"(c));
    asm("v_pk_fma_f32 %0, %1, %2, %3 op_sel:[1,1,0] op_sel_hi:[0,1,1] neg_lo:[1,0,0]" : "=v"(r) : "v"(a), "v"(w), "v"(t));
    return r;
}
template <int INV> __device__ __forceinline__ f2 ctw(f2 a, f2 w) { return INV ? cmulc(a, w) : cmul(a, w); }
template <int INV> __device__ __forceinline__ f2 add_rot(f2 a, f2 b) { return INV ? add_pi(a, b) : add_mi(a, b); }
template <int INV> __device__ __forceinline__ f2 sub_rot(f2 a, f2 b) { return INV ? add_mi(a, b) : add_pi(a, b); }
__device__ __forceinline__ f2 fma_mi(f2 a, f2 b, f2 ss) { f2 r; asm("v_pk_fma_f32 %0, %1, %2, %3 op_sel:[1,0,0] op_sel_hi:[0,1,1] neg_hi:[1,0,0]" : "=v"(r) : "v"(b), "v"(ss), "v"(a)); return r; }
__device__ __forceinline__ f2 fma_pi(f2 a, f2 b, f2 ss) { f2 r; asm("v_pk_fma_f32 %0, %1, %2, %3 op_sel:[1,0,0] op_sel_hi:[0,1,1] neg_lo:[1,0,0]" : "=v"(r) : "v"(b), "v"(ss), "v"(a)); return r; }
template <int INV> __device__ __forceinline__ f2 fma_rot(f2 a, f2 b, f2 ss)  { return INV ? fma_pi(a, b, ss) : fma_mi(a, b, ss); }
template <int INV> __device__ __forceinline__ f2 fms_rot(f2 a, f2 b, f2 ss)  { return INV ? fma_mi(a, b, ss) : fma_pi(a, b, ss); }

template <int INV> __device__ __forceinline__ void fft4(f2& a0, f2& a1, f2& a2, f2& a3) {
    f2 e0 = a0 + a2, e1 = a0 - a2, e2 = a1 + a3, d = a1 - a3;
    a0 = e0 + e2; a2 = e0 - e2; a1 = add_rot<INV>(e1, d); a3 = sub_rot<INV>(e1, d);
}
template <int INV> __device__ __forceinline__ void fft8(f2* a) {
    const f2 hh = {0.70710678118654752440f, 0.70710678118654752440f};
    f2 b0 = a[0] + a[4], b1 = a[1] + a[5], b2 = a[2] + a[6], b3 = a[3] + a[7];
    f2 c0 = a[0] - a[4], d1 = a[1] - a[5], d2 = a[2] - a[6], d3 = a[3] - a[7];
    fft4<INV>(b0, b1, b2, b3);
    f2 u1 = add_rot<INV>(d1, d1);
    f2 u3 = sub_rot<INV>(d3, d3);
    f2 e0 = add_rot<INV>(c0, d2), e1 = sub_rot<INV>(c0, d2);
    f2 e2 = u1 - u3;
    f2 dd = u1 + u3;
    a[0] = b0; a[2] = b1; a[4] = b2; a[6] = b3;
    a[1] = e0 + e2 * hh; a[5] = e0 - e2 * hh;
    a[3] = fma_rot<INV>(e1, dd, hh); a[7] = fms_rot<INV>(e1, dd, hh);
}
template <int INV> __device__ __forceinline__ void fft3(f2& a0, f2& a1, f2& a2) {
    const f2 ss = {0.86602540378443864676f, 0.86602540378443864676f};
    f2 t1 = a1 + a2, d = a1 - a2;
    f2 t2 = a0 - 0.5f * t1;
    a0 = a0 + t1; a1 = fma_rot<INV>(t2, d, ss); a2 = fms_rot<INV>(t2, d, ss);
}
template <int INV> __device__ __forceinline__ void fft9(f2* a) {
    const f2 w1 = {0.76604444311897803520f, -0.64278760968653932632f};
    const f2 w2 = {0.17364817766693034885f, -0.98480775301220805937f};
    const f2 w4 = {-0.93969262078590838405f, -0.34202014332566873304f};
    fft3<INV>(a[0], a[3], a[6]);
    fft3<INV>(a[1], a[4], a[7]);
    fft3<INV>(a[2], a[5], a[8]);
    a[4] = ctw<INV>(a[4], w1); a[7] = ctw<INV>(a[7], w2);
    a[5] = ctw<INV>(a[5], w2); a[8] = ctw<INV>(a[8], w4);
    fft3<INV>(a[0], a[1], a[2]);
    fft3<INV>(a[3], a[4], a[5]);
    fft3<INV>(a[6], a[7], a[8]);
    f2 x1 = a[3], x2 = a[6], x3 = a[1], x5 = a[7], x6 = a[2], x7 = a[5];
    a[1] = x1; a[2] = x2; a[3] = x3; a[5] = x5; a[6] = x6; a[7] = x7;
}

template <int CTRL> __device__ __forceinline__ float dppmov(float v) {
    return __builtin_bit_cast(float, __builtin_amdgcn_update_dpp(0, __builtin_bit_cast(int, v), CTRL, 0xF, 0xF, true));
}
__device__ __forceinline__ float sum32(float v) {
    v += dppmov<0xB1>(v);
    v += dppmov<0x4E>(v);
    v += dppmov<0x141>(v);
    v += dppmov<0x140>(v);
    float v2 = v;
    asm volatile("s_nop 1\n\tv_permlane16_swap_b32 %0, %1" : "+v"(v), "+v"(v2));
    return v + v2;
}

__device__ __forceinline__ void ln_row(const f2* v, f2* o, float& m_bf, float& rinv) {
    f2 s2 = v[0];
#pragma unroll
    for (int i = 1; i < 9; ++i) s2 += v[i];
    const float mean = sum32(s2.x + s2.y) * (1.0f / 576.0f);
    const f2 mm = {mean, mean};
    f2 q2 = {0.f, 0.f};
#pragma unroll
    for (int i = 0; i < 9; ++i) { f2 d = v[i] - mm; q2 += d * d; }
    const float var_bf = rbf(sum32(q2.x + q2.y) * (1.0f / 576.0f));
    m_bf = rbf(mean);
    const float den = rbf(__builtin_amdgcn_sqrtf(rbf(var_bf + 1e-5f)));
    rinv = __builtin_amdgcn_rcpf(den);
    const f2 mb = {m_bf, m_bf}, ri = {rinv, rinv};
#pragma unroll
    for (int i = 0; i < 9; ++i) o[i] = rbf2(rbf2(v[i] - mb) * ri);
}

template <int DOWN>
__device__ __forceinline__ u32 pf_issue(const u32* __restrict__ w, int b, int t) {
    const int x = b & 7, r = (b >> 3) & 31;
    const int e = min(t, 161);
    int off;
    if (!DOWN) { const int rr = e / 9, j = e - 9 * rr; off = ((18 * r + rr) * 2304 + (16 * j + 2 * x) * 16) * 4; }
    else       { const int j = e / 18, rr = e - 18 * j; off = ((16 * j + 2 * x) * 9216 + (18 * r + rr) * 32) * 4; }
    u32 d = 0;
    if (t < 192) asm volatile("global_load_dword %0, %1, %2" : "=v"(d) : "v"(off), "s"(w));
    return d;
}
__device__ __forceinline__ u32 pf_issue2(const u32* __restrict__ w, int b, int t) {
    const int x = b & 7, r = b >> 3;
    const int e = min(r * 273 + min(t, 272), 5183);
    const int row = e / 9, j = e - 9 * row;
    const int off = (row * 2304 + (16 * j + 2 * x) * 16) * 4;
    u32 d = 0;
    if (t < 320) asm volatile("global_load_dword %0, %1, %2" : "=v"(d) : "v"(off), "s"(w));
    return d;
}
__device__ __forceinline__ void pf_done(u32 d) { asm volatile("s_waitcnt vmcnt(0)" :: "v"(d) : "memory"); }

__global__ __launch_bounds__(320) void k1_ln_rowfft(const u32* __restrict__ x32, f2* __restrict__ T1,
                                                    const u16* __restrict__ wr, const u16* __restrict__ wi, u32x4* __restrict__ Wp,
                                                    const u32* __restrict__ pfw) {
    __shared__ f2 bufA[90 * 36];
    __shared__ f2 bufB[90 * 36];
    __shared__ __attribute__((aligned(16))) f2 tab[704];
    const int t = threadIdx.x, b = blockIdx.x;
    unsigned long long pc0;
    asm volatile("s_getpc_b64 %0" : "=s"(pc0));
    asm volatile("" :: "s"(T1), "s"(Wp));
    const int j = t >> 5, l = t & 31;
    const f4 tv0 = reinterpret_cast<const f4*>(g_tabR)[t], tv1 = reinterpret_cast<const f4*>(g_tabR)[320 + (t & 31)];
    __builtin_amdgcn_sched_barrier(0);
    u32 raw[9];
    f2 twf[8];
    {
        const u32* row = x32 + (size_t)(b + 256 * (j & 7)) * 288 + l;
#pragma unroll
        for (int i = 0; i < 9; ++i) raw[i] = ld32(row + 32 * i);
#pragma unroll
        for (int q = 1; q < 8; ++q) twf[q] = twF(b, q);
    }
    __builtin_amdgcn_sched_barrier(0);
    reinterpret_cast<f4*>(tab)[t] = tv0;
    reinterpret_cast<f4*>(tab)[320 + (t & 31)] = tv1;
    {
        f2 v[9], a[9]; float m_bf, rinv;
#pragma unroll
        for (int i = 0; i < 9; ++i) v[i] = unpack2(raw[i]);
        ln_row(v, a, m_bf, rinv);
        fft9<0>(a);
#pragma unroll
        for (int q = 0; q < 9; ++q) bufA[(j * 9 + q) * 36 + l] = a[q];
    }
    u32 wre[8], wim[8];
    {
        const int off0 = b * 289 + min(t, 288);
#pragma unroll
        for (int i = 0; i < 8; ++i) { wre[i] = ld16(wr + off0 + i * (256 * 289)); wim[i] = ld16(wi + off0 + i * (256 * 289)); }
    }
    const u32 pfd = pf_issue<0>(pfw, b, t);
    u32 pfc = 0;
    if (t >= 192 && t < 200) {
        const int i = (b >> 3) * 8 + (t - 192);
        if (i < 168) { const int off = i * 128; asm volatile("global_load_dword %0, %1, %2" : "=v"(pfc) : "v"(off), "s"(pc0)); }
        else if (i < 188) { const int off = (i - 168) * 128; asm volatile("global_load_dword %0, %1, %2" : "=v"(pfc) : "v"(off), "s"(g_tabC)); }
    }
    __syncthreads();
    {
        const int jq = t >> 2, lp = t & 3, q = jq % 9;
        f2 c[8];
#pragma unroll
        for (int i = 0; i < 8; ++i) c[i] = cmul(bufA[jq * 36 + lp + 4 * i], tab[TAB_A + q * 36 + lp + 4 * i]);
        fft8<0>(c);
#pragma unroll
        for (int q2 = 0; q2 < 8; ++q2) bufB[jq * 36 + 9 * lp + q2] = q2 ? cmul(c[q2], tab[TAB_B + q2 * 4 + lp]) : c[q2];
    }
    __syncthreads();
    {
        const int jj = t / 36, rem = t - 36 * jj, c = rem / 9, q = rem - 9 * c;
#pragma unroll
        for (int h = 0; h < 2; ++h) {
            const int q2 = c + 4 * h;
            const f2* src = &bufB[(jj * 9 + q) * 36 + q2];
            f2 d0 = src[0], d1 = src[9], d2 = src[18], d3 = src[27];
            fft4<0>(d0, d1, d2, d3);
            f2* dst = &bufA[jj * 288 + q + 9 * q2];
            dst[0] = d0; dst[72] = d1; dst[144] = d2; dst[216] = d3;
        }
    }
    __syncthreads();
    {
        const int k = t;
        const int ka = (k >= 288) ? 0 : k;
        const int kb = (ka == 0) ? 0 : 288 - ka;
        const f2 wk = tab[TAB_5 + k];
        f2 X[8];
#pragma unroll
        for (int jj = 0; jj < 8; ++jj) {
            f2 za = bufA[jj * 288 + ka], zb = bufA[jj * 288 + kb];
            f2 E = add_cj(za, zb), Dd = sub_cj(za, zb);
            X[jj] = cmac(E, Dd, f2{wk.y, -wk.x});
        }
        fft8<0>(X);
        if (k < 304) {
            f2* dst = T1 + ((size_t)(k >> 4) * 256 + b) * 16 + (k & 15);
#pragma unroll
            for (int p = 0; p < 8; ++p) ST(dst + (size_t)p * (19 * 4096), p ? cmul(X[p], twf[p]) : X[p]);
            const int pp = b & 7, q1 = (b >> 3) & 7, gp = b >> 6;
            u32x4* wd = Wp + ((((size_t)(pp * 19 + (k >> 4)) * 32 + (q1 * 4 + gp)) * 16 + (k & 15)) << 1);
            wd[0] = u32x4{wre[0] | (wim[0] << 16), wre[2] | (wim[2] << 16), wre[4] | (wim[4] << 16), wre[6] | (wim[6] << 16)};
            wd[1] = u32x4{wre[1] | (wim[1] << 16), wre[3] | (wim[3] << 16), wre[5] | (wim[5] << 16), wre[7] | (wim[7] << 16)};
        }
    }
    pf_done(pfd);
    asm volatile("" :: "v"(pfc));
}

__global__ __launch_bounds__(512) void k2_colfft(const f2* __restrict__ T1, const u32x4* __restrict__ Wp, f2* __restrict__ T2, const u32* __restrict__ pfw) {
    __shared__ f2 bufA[8 * 36 * 16];
    __shared__ f2 bufB[8 * 36 * 16];
    __shared__ __attribute__((aligned(16))) f2 tab[320];
    const int t = threadIdx.x, p = blockIdx.x & 7, kg = blockIdx.x >> 3;
    const int kl = t & 15, g = t >> 4;
    const int k = kg * 16 + kl;
    const int q1 = g >> 2, gp = g & 3;
    asm volatile("" :: "s"(T2), "s"(Wp));
    const f4 tv0 = reinterpret_cast<const f4*>(g_tabC)[min(t, 159)];
    __builtin_amdgcn_sched_barrier(0);
    const f2* src0 = T1 + (size_t)(p * 19 + kg) * 4096 + t;
    f2 a[8];
#pragma unroll
    for (int j = 0; j < 8; ++j) a[j] = ld64(src0 + j * 512);
    f2 wq[4];
    {
        const f2* wp = reinterpret_cast<const f2*>(Wp + (((size_t)(p * 19 + kg) * 512 + t) << 1));
#pragma unroll
        for (int i = 0; i < 4; ++i) wq[i] = ld64(wp + i);
    }
    __builtin_amdgcn_sched_barrier(0);
    reinterpret_cast<f4*>(tab)[min(t, 159)] = tv0;
    fft8<0>(a);
#pragma unroll
    for (int q = 0; q < 8; ++q) bufA[(q * 32 + g) * 16 + kl] = a[q];
    asm volatile("" : "+v"(wq[0]), "+v"(wq[1]), "+v"(wq[2]), "+v"(wq[3]));
    const u32 pfd = pf_issue2(pfw, blockIdx.x, t);
    __syncthreads();
    f2 t1[8], t2[8];
#pragma unroll
    for (int j = 0; j < 8; ++j) { t1[j] = tab[q1 * 36 + gp + 4 * j]; t2[j] = tab[288 + j * 4 + gp]; }
#pragma unroll
    for (int j = 0; j < 8; ++j) a[j] = cmul(bufA[(q1 * 32 + gp + 4 * j) * 16 + kl], t1[j]);
    fft8<0>(a);
#pragma unroll
    for (int q = 0; q < 8; ++q) bufB[(q1 * 36 + 9 * gp + q) * 16 + kl] = q ? cmul(a[q], t2[q]) : a[q];
    __syncthreads();
#pragma unroll
    for (int h = 0; h < 2; ++h) {
        const int q2 = gp + 4 * h;
        const f2* src = &bufB[(q1 * 36 + q2) * 16 + kl];
        f2 d0 = src[0], d1 = src[144], d2 = src[288], d3 = src[432];
        fft4<0>(d0, d1, d2, d3);
        const u32x2 wa = __builtin_bit_cast(u32x2, wq[2 * h]), wb = __builtin_bit_cast(u32x2, wq[2 * h + 1]);
        d0 = cmul(d0, unpack2(wa.x));
        d1 = cmul(d1, unpack2(wa.y));
        d2 = cmul(d2, unpack2(wb.x));
        d3 = cmul(d3, unpack2(wb.y));
        fft4<1>(d0, d1, d2, d3);
        f2* dst = &bufA[(q1 * 36 + q2) * 16 + kl];
        dst[0] = d0; dst[144] = d1; dst[288] = d2; dst[432] = d3;
    }
    __syncthreads();
#pragma unroll
    for (int q = 0; q < 8; ++q) { f2 v = bufA[(q1 * 36 + 9 * gp + q) * 16 + kl]; a[q] = q ? cmulc(v, t2[q]) : v; }
    fft8<1>(a);
#pragma unroll
    for (int j = 0; j < 8; ++j) bufB[(q1 * 32 + gp + 4 * j) * 16 + kl] = cmulc(a[j], t1[j]);
    __syncthreads();
#pragma unroll
    for (int q = 0; q < 8; ++q) a[q] = bufB[(q * 32 + g) * 16 + kl];
    fft8<1>(a);
    f2* base = T2 + (size_t)(g * 8 + p) * KS + k;
#pragma unroll
    for (int j = 0; j < 8; ++j) ST(base + (size_t)j * (32 * 8 * KS), a[j]);
    pf_done(pfd);
}

__global__ __launch_bounds__(320) void k3_irowfft_ln(const f2* __restrict__ T1, const u32* __restrict__ x32,
                                                     const u32* __restrict__ wr32, u32* __restrict__ out32,
                                                     u32* __restrict__ h32, f2* __restrict__ stats, u32* __restrict__ keys,
                                                     const u32* __restrict__ pfw) {
    __shared__ f2 bufA[90 * 36];
    __shared__ f2 bufB[90 * 36];
    __shared__ __attribute__((aligned(16))) f2 tab[704];
    const int t = threadIdx.x, b = blockIdx.x;
    asm volatile("" :: "s"(out32), "s"(h32), "s"(stats), "s"(keys));
    const int j = t >> 5, l = t & 31;
    const int jj = t / 36, rem = t - 36 * jj, c = rem / 9, q = rem - 9 * c;
    const f4 tv0 = reinterpret_cast<const f4*>(g_tabR)[t], tv1 = reinterpret_cast<const f4*>(g_tabR)[320 + (t & 31)];
    __builtin_amdgcn_sched_barrier(0);
    f2 X[8], twf[8];
    {
        const f2* src = T1 + (size_t)b * 8 * KS + min(t, 288);
#pragma unroll
        for (int p = 0; p < 8; ++p) X[p] = ld64(src + p * KS);
#pragma unroll
        for (int p = 1; p < 8; ++p) twf[p] = twF(b, p);
    }
    u32 raw[9], wv[9];
    {
        const u32* row = x32 + (size_t)(b + 256 * (j & 7)) * 288 + l;
#pragma unroll
        for (int i = 0; i < 9; ++i) { raw[i] = ld32(row + 32 * i); wv[i] = ld32(wr32 + l + 32 * i); }
    }
    __builtin_amdgcn_sched_barrier(0);
    reinterpret_cast<f4*>(tab)[t] = tv0;
    reinterpret_cast<f4*>(tab)[320 + (t & 31)] = tv1;
    {
        const int k = t;
#pragma unroll
        for (int p = 1; p < 8; ++p) X[p] = cmulc(X[p], twf[p]);
        fft8<1>(X);
        const f2 ym = {1.0f, (k * (288 - k) != 0) ? 1.0f : 0.0f};
#pragma unroll
        for (int i = 0; i < 8; ++i) bufA[i * 320 + k] = X[i] * ym;
    }
    asm volatile("" : "+v"(raw[0]), "+v"(raw[1]), "+v"(raw[2]), "+v"(raw[3]), "+v"(raw[4]), "+v"(raw[5]), "+v"(raw[6]), "+v"(raw[7]), "+v"(raw[8]),
                      "+v"(wv[0]), "+v"(wv[1]), "+v"(wv[2]), "+v"(wv[3]), "+v"(wv[4]), "+v"(wv[5]), "+v"(wv[6]), "+v"(wv[7]), "+v"(wv[8]));
    const u32 pfd = pf_issue<1>(pfw, b, t);
    __syncthreads();
    {
        const int jc = min(jj, 7);
#pragma unroll
        for (int h = 0; h < 2; ++h) {
            const int q2 = c + 4 * h;
            f2 d[4];
#pragma unroll
            for (int k3 = 0; k3 < 4; ++k3) {
                const int k = q + 9 * q2 + 72 * k3;
                f2 xa = bufA[jc * 320 + k], xb = bufA[jc * 320 + 288 - k];
                f2 E = add_cj(xa, xb), Dd = sub_cj(xa, xb);
                const f2 w = tab[TAB_5 + k];
                d[k3] = cmac(E, Dd, f2{w.y, w.x});
            }
            fft4<1>(d[0], d[1], d[2], d[3]);
            f2* dst = &bufB[(jj * 9 + q) * 36 + q2];
            dst[0] = d[0]; dst[9] = d[1]; dst[18] = d[2]; dst[27] = d[3];
        }
    }
    __syncthreads();
    {
        const int jq = t >> 2, lp = t & 3, qq = jq % 9;
        f2 cc[8];
#pragma unroll
        for (int q2 = 0; q2 < 8; ++q2) { f2 v = bufB[jq * 36 + 9 * lp + q2]; cc[q2] = q2 ? cmulc(v, tab[TAB_B + q2 * 4 + lp]) : v; }
        fft8<1>(cc);
#pragma unroll
        for (int i = 0; i < 8; ++i) bufA[jq * 36 + lp + 4 * i] = cmulc(cc[i], tab[TAB_A + qq * 36 + lp + 4 * i]);
    }
    __syncthreads();
    if (t < 256) {
        const int s = b + 256 * j;
        f2 a[9];
#pragma unroll
        for (int qq = 0; qq < 9; ++qq) a[qq] = bufA[(j * 9 + qq) * 36 + l];
        fft9<1>(a);
        const f2 sc = {1.0f / 2359296.0f, 1.0f / 2359296.0f};
        f2 hv[9], o[9]; float m_bf, rinv;
        u32 hp[9];
#pragma unroll
        for (int i = 0; i < 9; ++i) { hp[i] = pack2(unpack2(raw[i]) + rbf2(a[i] * sc)); hv[i] = unpack2(hp[i]); }
        u32* hrow = h32 + (size_t)s * 288 + l;
#pragma unroll
        for (int i = 0; i < 9; ++i) __hip_atomic_store(hrow + 32 * i, hp[i], __ATOMIC_RELAXED, __HIP_MEMORY_SCOPE_AGENT);
        ln_row(hv, o, m_bf, rinv);
        u32* orow = out32 + (size_t)s * 288 + l;
        f2 r2 = {0.f, 0.f};
#pragma unroll
        for (int i = 0; i < 9; ++i) { __hip_atomic_store(orow + 32 * i, pack2(hv[i] + o[i]), __ATOMIC_RELAXED, __HIP_MEMORY_SCOPE_AGENT); r2 += o[i] * unpack2(wv[i]); }
        const float r = sum32(r2.x + r2.y);
        if (l == 0) {
            u32 rb = bfbits(r);
            u32 u = (rb & 0x8000u) ? (rb ^ 0xFFFFu) : (rb | 0x8000u);
            keys[s] = (u << 16) | (u32)(0xFFFF - s);
            stats[s] = f2{m_bf, rinv};
        }
    }
    pf_done(pfd);
}

__device__ __forceinline__ u32 wave_max_u32(u32 v) {
    v = max(v, (u32)__builtin_amdgcn_update_dpp(0, (int)v, 0xB1, 0xF, 0xF, true));
    v = max(v, (u32)__builtin_amdgcn_update_dpp(0, (int)v, 0x4E, 0xF, 0xF, true));
    v = max(v, (u32)__builtin_amdgcn_update_dpp(0, (int)v, 0x141, 0xF, 0xF, true));
    v = max(v, (u32)__builtin_amdgcn_update_dpp(0, (int)v, 0x140, 0xF, 0xF, true));
    u32 v2 = v;
    asm volatile("s_nop 1\n\tv_permlane16_swap_b32 %0, %1" : "+v"(v), "+v"(v2));
    v = max(v, v2); v2 = v;
    asm volatile("s_nop 1\n\tv_permlane32_swap_b32 %0, %1" : "+v"(v), "+v"(v2));
    return max(v, v2);
}

__global__ __launch_bounds__(832) void k4_ffn(const u32* __restrict__ keys, const u32* __restrict__ h32, const f2* __restrict__ stats,
                                              const u32* __restrict__ wg32, const u32* __restrict__ wu32, const u32x2* __restrict__ wd64,
                                              float* __restrict__ part, u32* __restrict__ selout, u32* __restrict__ hsel, u32* __restrict__ lnsel) {
    __shared__ u32 cand[32];
    __shared__ __attribute__((aligned(16))) u32 xs[8 * 292];
    __shared__ float red[9 * 2 * 8 * 32];
    __shared__ __attribute__((aligned(16))) u16 gu[8 * 32];
    const int t = threadIdx.x, l = t & 63, g = l >> 4, c = l & 15;
    const int w = __builtin_amdgcn_readfirstlane(t >> 6);
    const int blk = blockIdx.x;
    const int f0 = (16 * (blk >> 4) + 2 * (blk & 7) + ((blk >> 3) & 1)) * 32;
    asm volatile("" :: "s"(h32), "s"(stats), "s"(part), "s"(selout), "s"(hsel), "s"(lnsel));

    if (w >= 9) {
        const int cw = w - 9;
        const u32x4 ka = *reinterpret_cast<const u32x4*>(keys + cw * 512 + 4 * l);
        const u32x4 kb = *reinterpret_cast<const u32x4*>(keys + cw * 512 + 256 + 4 * l);
        u32 k0 = ka.x, k1 = ka.y, k2 = ka.z, k3 = ka.w, k4 = kb.x, k5 = kb.y, k6 = kb.z, k7 = kb.w, tmp;
#define CSWAP(a, b) tmp = max(a, b); b = min(a, b); a = tmp;
        CSWAP(k0, k2) CSWAP(k1, k3) CSWAP(k4, k6) CSWAP(k5, k7)
        CSWAP(k0, k4) CSWAP(k1, k5) CSWAP(k2, k6) CSWAP(k3, k7)
        CSWAP(k0, k1) CSWAP(k2, k3) CSWAP(k4, k5) CSWAP(k6, k7)
        CSWAP(k2, k4) CSWAP(k3, k5)
        CSWAP(k1, k4) CSWAP(k3, k6)
        CSWAP(k1, k2) CSWAP(k3, k4) CSWAP(k5, k6)
#undef CSWAP
        u32 mine = 0;
#pragma unroll
        for (int r = 0; r < 8; ++r) {
            const u32 m = wave_max_u32(k0);
            const bool hit = (k0 == m);
            k0 = hit ? k1 : k0; k1 = hit ? k2 : k1; k2 = hit ? k3 : k2; k3 = hit ? k4 : k3;
            k4 = hit ? k5 : k4; k5 = hit ? k6 : k5; k6 = hit ? k7 : k6;
            mine = (l == r) ? m : mine;
        }
        if (l < 8) cand[cw * 8 + l] = mine;
        __syncthreads();
        u32 selA = 0, selB = 0;
        {
            u32 kk = cand[l & 31];
#pragma unroll
            for (int r = 0; r < 8; ++r) {
                const u32 m = wave_max_u32(kk);
                kk = (kk == m) ? 0u : kk;
                selA = (2 * cw == r) ? m : selA;
                selB = (2 * cw + 1 == r) ? m : selB;
            }
        }
        const int sA = 0xFFFF - (int)(selA & 0xFFFFu), sB = 0xFFFF - (int)(selB & 0xFFFFu);
        const u32x4* hA = reinterpret_cast<const u32x4*>(h32 + (size_t)sA * 288);
        const u32x4* hB = reinterpret_cast<const u32x4*>(h32 + (size_t)sB * 288);
        const u32x4 ha = hA[l], hb = hB[l];
        const u32x4 hc = (l & 8) ? hB[64 + (l & 7)] : hA[64 + (l & 7)];
        const f2 stA = stats[sA], stB = stats[sB];
        const f2 stC = (l & 8) ? stB : stA;
        u32x4 la, lb, lc;
#pragma unroll
        for (int i = 0; i < 4; ++i) {
            la[i] = pack2(rbf2(unpack2(ha[i]) - f2{stA.x, stA.x}) * f2{stA.y, stA.y});
            lb[i] = pack2(rbf2(unpack2(hb[i]) - f2{stB.x, stB.x}) * f2{stB.y, stB.y});
            lc[i] = pack2(rbf2(unpack2(hc[i]) - f2{stC.x, stC.x}) * f2{stC.y, stC.y});
        }
        const int tc = 2 * cw + ((l >> 3) & 1);
        *reinterpret_cast<u32x4*>(&xs[(2 * cw) * 292 + 4 * l]) = la;
        *reinterpret_cast<u32x4*>(&xs[(2 * cw + 1) * 292 + 4 * l]) = lb;
        if (l < 16) *reinterpret_cast<u32x4*>(&xs[tc * 292 + 256 + 4 * (l & 7)]) = lc;
        __syncthreads();
        if (blk == 0) {
            reinterpret_cast<u32x4*>(hsel + (2 * cw) * 288)[l] = ha;
            reinterpret_cast<u32x4*>(hsel + (2 * cw + 1) * 288)[l] = hb;
            reinterpret_cast<u32x4*>(lnsel + (2 * cw) * 288)[l] = la;
            reinterpret_cast<u32x4*>(lnsel + (2 * cw + 1) * 288)[l] = lb;
            if (l < 16) { reinterpret_cast<u32x4*>(hsel + tc * 288)[64 + (l & 7)] = hc; reinterpret_cast<u32x4*>(lnsel + tc * 288)[64 + (l & 7)] = lc; }
            if (l == 0) { selout[2 * cw] = selA; selout[2 * cw + 1] = selB; }
        }
        return;
    }
    __builtin_amdgcn_s_sleep(WSL);
    u32 vg[2][8], vu[2][8];
    u32x2 vd[8];
    {
        const int off0 = (64 * w + 8 * g) * 2304 + (f0 >> 1) + c;
#pragma unroll
        for (int s = 0; s < 2; ++s)
#pragma unroll
            for (int j = 0; j < 8; ++j) {
                vg[s][j] = ld32(wg32 + off0 + (32 * s + j) * 2304);
                vu[s][j] = ld32(wu32 + off0 + (32 * s + j) * 2304);
            }
        const int offd = (f0 + 8 * g) * 144 + w * 16 + c;
#pragma unroll
        for (int j = 0; j < 8; ++j) vd[j] = __builtin_bit_cast(u32x2, ld64(reinterpret_cast<const f2*>(wd64 + offd + j * 144)));
    }
    __builtin_amdgcn_sched_barrier(0);
    __syncthreads();
    __syncthreads();
    f4 accg[2] = {f4{0, 0, 0, 0}, f4{0, 0, 0, 0}}, accu[2] = {f4{0, 0, 0, 0}, f4{0, 0, 0, 0}};
#pragma unroll
    for (int s = 0; s < 2; ++s) {
        const u32x4 av = *reinterpret_cast<const u32x4*>(&xs[(c & 7) * 292 + 16 * (2 * w + s) + 4 * g]);
        const s8 A = __builtin_bit_cast(s8, av);
        u32x4 b0, b1;
#pragma unroll
        for (int i = 0; i < 4; ++i) {
            b0[i] = __builtin_amdgcn_perm(vg[s][2 * i + 1], vg[s][2 * i], 0x05040100u);
            b1[i] = __builtin_amdgcn_perm(vg[s][2 * i + 1], vg[s][2 * i], 0x07060302u);
        }
        accg[0] = __builtin_amdgcn_mfma_f32_16x16x32_bf16(A, __builtin_bit_cast(s8, b0), accg[0], 0, 0, 0);
        accg[1] = __builtin_amdgcn_mfma_f32_16x16x32_bf16(A, __builtin_bit_cast(s8, b1), accg[1], 0, 0, 0);
#pragma unroll
        for (int i = 0; i < 4; ++i) {
            b0[i] = __builtin_amdgcn_perm(vu[s][2 * i + 1], vu[s][2 * i], 0x05040100u);
            b1[i] = __builtin_amdgcn_perm(vu[s][2 * i + 1], vu[s][2 * i], 0x07060302u);
        }
        accu[0] = __builtin_amdgcn_mfma_f32_16x16x32_bf16(A, __builtin_bit_cast(s8, b0), accu[0], 0, 0, 0);
        accu[1] = __builtin_amdgcn_mfma_f32_16x16x32_bf16(A, __builtin_bit_cast(s8, b1), accu[1], 0, 0, 0);
    }
    if (l < 32) {
#pragma unroll
        for (int r = 0; r < 4; ++r) {
            const int tk = 4 * g + r;
            float* dg = &red[((w * 2 + 0) * 8 + tk) * 32 + 2 * c];
            float* du = &red[((w * 2 + 1) * 8 + tk) * 32 + 2 * c];
            *reinterpret_cast<f2*>(dg) = f2{accg[0][r], accg[1][r]};
            *reinterpret_cast<f2*>(du) = f2{accu[0][r], accu[1][r]};
        }
    }
    __syncthreads();
    if (t < 256) {
        float gs = 0.f, us = 0.f;
#pragma unroll
        for (int i = 0; i < 9; ++i) { gs += red[(i * 2 + 0) * 256 + t]; us += red[(i * 2 + 1) * 256 + t]; }
        const float gb = rbf(gs), ub = rbf(us);
        const float sl = rbf(gb / (1.0f + __expf(-gb)));
        gu[t] = (u16)bfbits(sl * ub);
    }
    __syncthreads();
    {
        const u32x4 av = *reinterpret_cast<const u32x4*>(&gu[(c & 7) * 32 + 8 * g]);
        const s8 A = __builtin_bit_cast(s8, av);
        f4 acc[4];
#pragma unroll
        for (int n = 0; n < 4; ++n) {
            u32x4 bb;
#pragma unroll
            for (int i = 0; i < 4; ++i) {
                const u32 lo = (n < 2) ? vd[2 * i].x : vd[2 * i].y;
                const u32 hi = (n < 2) ? vd[2 * i + 1].x : vd[2 * i + 1].y;
                bb[i] = __builtin_amdgcn_perm(hi, lo, (n & 1) ? 0x07060302u : 0x05040100u);
            }
            acc[n] = __builtin_amdgcn_mfma_f32_16x16x32_bf16(A, __builtin_bit_cast(s8, bb), f4{0, 0, 0, 0}, 0, 0, 0);
        }
        if (l < 32) {
#pragma unroll
            for (int r = 0; r < 4; ++r) {
                const int tk = 4 * g + r;
                *reinterpret_cast<f4*>(&part[((size_t)blk * 8 + tk) * 576 + 64 * w + 4 * c]) =
                    f4{acc[0][r], acc[1][r], acc[2][r], acc[3][r]};
            }
        }
    }
}

__global__ __launch_bounds__(256) void k5_final(const float* __restrict__ part, const u32* __restrict__ selin,
                                                const u32* __restrict__ hsel, const u32* __restrict__ lnsel, u32* __restrict__ out32) {
    __shared__ f4 red[256];
    const int t = threadIdx.x, tk = blockIdx.x / 9, cg = blockIdx.x - 9 * tk;
    const int c4 = t & 15, pg = t >> 4;
    f4 pv[9];
#pragma unroll
    for (int i = 0; i < 9; ++i)
        pv[i] = *reinterpret_cast<const f4*>(&part[((size_t)(pg * 9 + i) * 8 + tk) * 576 + 64 * cg + 4 * c4]);
    const u32 key = selin[tk];
    const u32x2 hv = *reinterpret_cast<const u32x2*>(&hsel[tk * 288 + 32 * cg + 2 * c4]);
    const u32x2 lv = *reinterpret_cast<const u32x2*>(&lnsel[tk * 288 + 32 * cg + 2 * c4]);
    __builtin_amdgcn_sched_barrier(0);
    f4 s = pv[0];
#pragma unroll
    for (int i = 1; i < 9; ++i) s += pv[i];
    red[t] = s;
    const int srow = 0xFFFF - (int)(key & 0xFFFFu);
    const u32 u = key >> 16;
    const u32 rb = (u & 0x8000u) ? (u ^ 0x8000u) : (u ^ 0xFFFFu);
    float sg = rbf(1.0f / (1.0f + __expf(-lo16f(rb))));
    asm volatile("" : "+v"(sg));
    __syncthreads();
    if (t < 16) {
        f4 y = red[t];
#pragma unroll
        for (int i = 1; i < 16; ++i) y += red[t + 16 * i];
        const f2 sg2 = {sg, sg};
        const f2 ya = rbf2(rbf2(f2{y[0], y[1]}) * sg2), yb = rbf2(rbf2(f2{y[2], y[3]}) * sg2);
        u32x2 ov;
        ov.x = pack2(unpack2(hv.x) + rbf2(unpack2(lv.x) + ya));
        ov.y = pack2(unpack2(hv.y) + rbf2(unpack2(lv.y) + yb));
        *reinterpret_cast<u32x2*>(&out32[(size_t)srow * 288 + 32 * cg + 2 * t]) = ov;
    }
}

extern "C" void kernel_launch(void* const* d_in, const int* in_sizes, int n_in,
                              void* d_out, int out_size, void* d_ws, size_t ws_size,
                              hipStream_t stream) {
    const u32* x32  = (const u32*)d_in[0];
    const u16* wr   = (const u16*)d_in[1];
    const u16* wi   = (const u16*)d_in[2];
    const u32* rw32 = (const u32*)d_in[4];
    const u32* wg32 = (const u32*)d_in[5];
    const u32* wu32 = (const u32*)d_in[6];
    const u32x2* wd64 = (const u32x2*)d_in[7];
    char* ws = (char*)d_ws;
    f2* T1     = (f2*)(ws);
    u32* h32   = (u32*)(ws + (5u << 20));
    f2* stats  = (f2*)(ws + (8u << 20));
    u32* keys  = (u32*)(ws + (8u << 20) + 65536);
    u32* sel   = (u32*)(ws + (8u << 20) + 131072);
    u32* hsel  = (u32*)(ws + (8u << 20) + 196608);
    u32* lnsel = (u32*)(ws + (8u << 20) + 262144);
    f2* T2 = (f2*)(ws + (12u << 20));
    u32x4* Wp = (u32x4*)(ws + (17u << 20));
    float* part = (float*)(ws + (9u << 20));
    u32* out32 = (u32*)d_out;

    k1_ln_rowfft<<<256, 320, 0, stream>>>(x32, T1, wr, wi, Wp, wg32);
    k2_colfft<<<152, 512, 0, stream>>>(T1, Wp, T2, wu32);
    k3_irowfft_ln<<<256, 320, 0, stream>>>(T2, x32, rw32, out32, h32, stats, keys, (const u32*)wd64);
    k4_ffn<<<NPART, 832, 0, stream>>>(keys, h32, stats, wg32, wu32, wd64, part, sel, hsel, lnsel);
    k5_final<<<72, 256, 0, stream>>>(part, sel, hsel, lnsel, out32);
}
```
